```python
import torch

def _softmax(x, axis=-1):
    return torch.softmax(x, dim=axis)
def _gelu(x):
    return torch.nn.functional.gelu(x, approximate="tanh")
def _silu(x):
    return torch.nn.functional.silu(x)
def _sigmoid(x):
    return torch.sigmoid(x)
def _layernorm(x, axis=-1, eps=1e-5):
    m = x.mean(dim=axis, keepdim=True)
    v = x.var(dim=axis, keepdim=True, unbiased=False)
    return (x - m) / torch.sqrt(v + eps)
def _scatter_add(tgt, idx, src, axis=0):
    return tgt.clone().index_add_(axis, idx, src)
def _segment_sum(src, seg, n):
    out = torch.zeros((n,) + src.shape[1:], dtype=src.dtype)
    return out.index_add_(0, seg, src)
def _pad(x, pw):
    flat = tuple(v for pair in reversed(pw) for v in pair)
    return torch.nn.functional.pad(x, flat)

kernel_name = 'standalone__mha_block_cached__near_ridge'

def setup_inputs():

    _w = (lambda a: (lambda t: t.to(torch.bfloat16) if t.is_floating_point() else t)(torch.as_tensor(a)))
    B, Sq, D = 8, 128, 448
    H, Sk, Dh = 16, 512, 256
    return {
        "x": _w((lambda *s: torch.randn(*s, dtype=torch.bfloat16) * 0.8)(B, Sq, D)),
        "k_cache": _w((lambda *s: torch.randn(*s, dtype=torch.bfloat16) * 0.8)(B, H, Sk, Dh)),
        "v_cache": _w((lambda *s: torch.randn(*s, dtype=torch.bfloat16) * 0.05)(B, H, Sk, Dh)),
        "wq": _w((lambda *s: torch.randn(*s, dtype=torch.bfloat16) * 0.8)(D, H * Dh) * (D ** -0.5)),
        "wo": _w((lambda *s: torch.randn(*s, dtype=torch.bfloat16) * 0.05)(H * Dh, D) * ((H * Dh) ** -0.5)),
    }

def reference(x, k_cache, v_cache, wq, wo):
    B, Sq, D = x.shape
    H, Dh = 16, 256
    q = (lambda x, *d: x.permute(*d))((x @ wq).reshape(B, Sq, H, Dh), 0, 2, 1, 3)
    scores = torch.einsum("bhqd,bhkd->bhqk", q, k_cache) * (Dh ** -0.5)
    p = _softmax(scores, axis=-1)
    o = torch.einsum("bhqk,bhkd->bhqd", p, v_cache)
    return (lambda x, *d: x.permute(*d))(o, 0, 2, 1, 3).reshape(B, Sq, H * Dh) @ wo
```

```cpp
#include <hip/hip_runtime.h>
#include <hip/hip_bf16.h>
#include <stdint.h>
#include <type_traits>

typedef __bf16 bf16x8 __attribute__((ext_vector_type(8)));
typedef __bf16 bf16x2 __attribute__((ext_vector_type(2)));
typedef short s16x4 __attribute__((ext_vector_type(4)));
typedef short s16x8 __attribute__((ext_vector_type(8)));
typedef float f32x2 __attribute__((ext_vector_type(2)));
typedef float f32x16 __attribute__((ext_vector_type(16)));
typedef unsigned u32x2 __attribute__((ext_vector_type(2)));
typedef unsigned u32x4 __attribute__((ext_vector_type(4)));
#define LAS __attribute__((address_space(3)))
#define DI __device__ __forceinline__

constexpr int NSTEP = 31;
constexpr int NSLOT = 4;
constexpr unsigned SLOT_BYTES = 32768;
constexpr unsigned SCR = NSLOT * SLOT_BYTES;

struct Sched { int slot[NSTEP]; int wait[NSTEP]; int issue_at[NSTEP]; int pro[NSLOT]; };
constexpr Sched make_sched() {
  Sched s{};
  int ord[NSTEP] = {};
  for (int i = 0; i < NSTEP; ++i) ord[i] = i;
  int pos[NSTEP] = {};
  for (int i = 0; i < NSTEP; ++i) pos[ord[i]] = i;
  for (int i = 0; i < NSLOT; ++i) { s.pro[i] = ord[i]; s.slot[ord[i]] = i; }
  for (int t = 0; t < NSTEP; ++t) {
    int last = (t == 0) ? (NSLOT - 1) : (t + NSLOT - 2 < NSTEP - 1 ? t + NSLOT - 2 : NSTEP - 1);
    s.wait[t] = 8 * (last - pos[t]);
    s.issue_at[t] = -1;
    if (t >= 1 && t + NSLOT - 1 < NSTEP) { int e = ord[t + NSLOT - 1]; s.issue_at[t] = e; s.slot[e] = s.slot[t - 1]; }
  }
  return s;
}
constexpr Sched SCH = make_sched();

template <int B, int E, class F> DI void static_for(F&& f) {
  if constexpr (B < E) { f(std::integral_constant<int, B>{}); static_for<B + 1, E>(f); }
}

DI void dma16(unsigned lds_addr, const char* sbase, unsigned voff) {
  asm volatile("s_mov_b32 m0, %0\n\ts_nop 0\n\tglobal_load_lds_dwordx4 %1, %2"
               :: "s"(lds_addr), "v"(voff), "s"(sbase) : "memory");
}
template <int N> DI void top_sync() {
  asm volatile("s_waitcnt vmcnt(%0) lgkmcnt(0)\n\ts_barrier" :: "n"(N) : "memory");
}
DI void lds_sync() { asm volatile("s_waitcnt lgkmcnt(0)\n\ts_barrier" ::: "memory"); }

DI unsigned pk2(float a, float b) {
  f32x2 v = {a, b};
  bf16x2 r = __builtin_convertvector(v, bf16x2);
  return __builtin_bit_cast(unsigned, r);
}
DI bf16x8 tr_frag(unsigned a0, unsigned a1) {
  s16x4 lo = __builtin_amdgcn_ds_read_tr16_b64_v4i16((LAS s16x4*)a0);
  s16x4 hi = __builtin_amdgcn_ds_read_tr16_b64_v4i16((LAS s16x4*)a1);
  s16x8 r = __builtin_shufflevector(lo, hi, 0, 1, 2, 3, 4, 5, 6, 7);
  return __builtin_bit_cast(bf16x8, r);
}
DI bf16x8 lds_b128(unsigned a) { return *(LAS const bf16x8*)a; }
DI float swap32_max(float v) {
  auto rr = __builtin_amdgcn_permlane32_swap(__float_as_uint(v), __float_as_uint(v), false, false);
  return fmaxf(__uint_as_float(rr[0]), __uint_as_float(rr[1]));
}
DI float swap32_add(float v) {
  auto rr = __builtin_amdgcn_permlane32_swap(__float_as_uint(v), __float_as_uint(v), false, false);
  return __uint_as_float(rr[0]) + __uint_as_float(rr[1]);
}

__global__ __launch_bounds__(256, 1) void attn_kernel(const char* __restrict__ x, const char* __restrict__ Kc,
                                                      const char* __restrict__ Vc, const char* __restrict__ wq,
                                                      const char* __restrict__ wo, char* __restrict__ slab) {
  __shared__ __attribute__((aligned(1024))) char smem[163840];
  const unsigned lds0 = (unsigned)(size_t)smem;
  const int tid = threadIdx.x, lane = tid & 63;
  const int wid = __builtin_amdgcn_readfirstlane(tid >> 6);
  const int qb = wid & 1, role = wid >> 1;
  const int hi = lane >> 5, l31 = lane & 31;
  const int bid = blockIdx.x;
  const int xcd = bid & 7, idx = bid >> 3;
  const int qh = idx & 1, hl = (idx >> 1) & 1, b = idx >> 2, h = xcd * 2 + hl;

  bf16x8 xf[28];
  {
    const char* xrow = x + ((size_t)(b * 128 + qh * 64 + qb * 32 + l31) * 448 + 8 * hi) * 2;
#pragma unroll
    for (int s = 0; s < 28; ++s) xf[s] = *(const bf16x8*)(xrow + 32 * s);
  }

  const char* Kb = Kc + (size_t)(b * 16 + h) * (512 * 512) + (size_t)((wid >> 1) * 256 + (wid & 1) * 16) * 512;
  const char* Vb = Vc + (size_t)(b * 16 + h) * (512 * 512) + (size_t)((wid >> 1) * 256 + (wid & 1) * 16) * 512;
  const char* Wqb = wq + (size_t)(wid * 16) * 8192 + h * 512;
  const char* Wob = wo + (size_t)(h * 256 + wid * 8) * 896;
  const unsigned ldsw = lds0 + wid * 8192;

  auto issue = [&](auto sc) {
    constexpr int S = decltype(sc)::value;
    constexpr unsigned sl = SCH.slot[S] * SLOT_BYTES;
    static_for<0, 8>([&](auto jc) {
      constexpr int j = decltype(jc)::value;
      if constexpr (S < 7) {
        unsigned voff = hi * 8192 + ((l31 ^ ((2 * (j & 1) + hi) << 2)) << 4);
        dma16(ldsw + sl + j * 1024, Wqb + (size_t)(64 * S + 2 * j) * 8192, voff);
      } else if constexpr (S < 15) {
        unsigned voff = hi * 512 + ((l31 ^ hi ^ (2 * j)) << 4);
        dma16(ldsw + sl + j * 1024, Kb + (size_t)(32 * (S - 7) + 2 * j) * 512, voff);
      } else if constexpr (S < 23) {
        unsigned voff = hi * 512 + ((l31 ^ ((2 * (j & 1) + hi) << 2)) << 4);
        dma16(ldsw + sl + j * 1024, Vb + (size_t)(32 * (S - 15) + 2 * j) * 512, voff);
      } else {
        unsigned g = lane ^ ((j & 3) << 2);
        g = (g >= 56) ? g - 8 : g;
        dma16(ldsw + sl + j * 1024, Wob + (size_t)(32 * (S - 23) + j) * 896, g << 4);
      }
    });
  };
  auto top = [&](auto sc) {
    constexpr int S = decltype(sc)::value;
    top_sync<SCH.wait[S]>();
    if constexpr (SCH.issue_at[S] >= 0) issue(std::integral_constant<int, SCH.issue_at[S]>{});
  };

  static_for<0, NSLOT>([&](auto ic) { issue(std::integral_constant<int, SCH.pro[decltype(ic)::value]>{}); });
#pragma unroll
  for (int s = 0; s < 28; ++s) asm volatile("" : "+v"(xf[s]));

  const int q4 = (lane & 15) >> 2, p = lane & 3, bb = (lane >> 4) & 1;
  unsigned trc[4];
#pragma unroll
  for (int blk = 0; blk < 4; ++blk) trc[blk] = ((16 * role + 4 * (blk ^ q4) + 2 * bb + (p >> 1)) << 4) + 8 * (p & 1);

  f32x16 acc[4];
#pragma unroll
  for (int i = 0; i < 4; ++i) acc[i] = f32x16{};
  static_for<0, 7>([&](auto tc) {
    constexpr int t = decltype(tc)::value;
    top(std::integral_constant<int, t>{});
    constexpr unsigned sl = SCH.slot[t] * SLOT_BYTES;
    const unsigned rb = lds0 + sl + (8 * hi + q4) * 512;
#pragma unroll
    for (int ks = 0; ks < 4; ++ks)
#pragma unroll
      for (int blk = 0; blk < 4; ++blk) {
        bf16x8 a = tr_frag(rb + trc[blk] + (16 * ks) * 512, rb + trc[blk] + (16 * ks + 4) * 512);
        acc[blk] = __builtin_amdgcn_mfma_f32_32x32x16_bf16(a, xf[4 * t + ks], acc[blk], 0, 0, 0);
      }
  });
  const unsigned qrow = lds0 + SCR + (32 * qb + l31) * 512;
#pragma unroll
  for (int blk = 0; blk < 4; ++blk)
#pragma unroll
    for (int g = 0; g < 4; ++g) {
      u32x2 w = {pk2(acc[blk][4 * g], acc[blk][4 * g + 1]), pk2(acc[blk][4 * g + 2], acc[blk][4 * g + 3])};
      *(LAS u32x2*)(qrow + (((16 * role + 4 * blk + g) ^ (l31 & 15)) << 4) + 8 * hi) = w;
    }

  f32x16 S[8];
  bf16x8 qf[16];
  float mx = -3.0e38f;
  static_for<0, 8>([&](auto tc) {
    constexpr int t = decltype(tc)::value;
    top(std::integral_constant<int, 7 + t>{});
    if constexpr (t == 0) {
#pragma unroll
      for (int s = 0; s < 16; ++s) qf[s] = lds_b128(qrow + (((2 * s + hi) ^ (l31 & 15)) << 4));
    }
    constexpr unsigned sl = SCH.slot[7 + t] * SLOT_BYTES;
    const unsigned kr = lds0 + sl + (32 * role + l31) * 512;
    f32x16 c = f32x16{};
#pragma unroll
    for (int s = 0; s < 16; ++s) {
      bf16x8 a = lds_b128(kr + (((2 * s + hi) ^ (l31 & 15)) << 4));
      c = __builtin_amdgcn_mfma_f32_32x32x16_bf16(a, qf[s], c, 0, 0, 0);
    }
    S[t] = c;
#pragma unroll
    for (int r = 0; r < 16; ++r) mx = fmaxf(mx, c[r]);
  });
  mx = swap32_max(mx);
  const unsigned xbuf = lds0 + SCR + 16384;
  *(LAS float*)(xbuf + wid * 256 + lane * 4) = mx;
  lds_sync();
  mx = fmaxf(mx, *(LAS const float*)(xbuf + (wid ^ 2) * 256 + lane * 4));

  constexpr float C = 0.0625f * 1.44269504088896340736f;
  const float mC = -mx * C;
  float lsum = 0.f;
  const unsigned pbuf = lds0 + SCR;
  bf16x8 pown[2];
  auto make_p = [&](const f32x16& s, int par) {
    float pe[16];
#pragma unroll
    for (int r = 0; r < 16; ++r) { pe[r] = __builtin_amdgcn_exp2f(fmaf(s[r], C, mC)); lsum += pe[r]; }
#pragma unroll
    for (int sp = 0; sp < 2; ++sp) {
      u32x4 w = {pk2(pe[8 * sp], pe[8 * sp + 1]), pk2(pe[8 * sp + 2], pe[8 * sp + 3]),
                 pk2(pe[8 * sp + 4], pe[8 * sp + 5]), pk2(pe[8 * sp + 6], pe[8 * sp + 7])};
      pown[sp] = __builtin_bit_cast(bf16x8, w);
      *(LAS u32x4*)(pbuf + par * 8192 + wid * 2048 + sp * 1024 + lane * 16) = w;
    }
  };
  make_p(S[0], 0);

  f32x16 o[4];
#pragma unroll
  for (int i = 0; i < 4; ++i) o[i] = f32x16{};
  static_for<0, 8>([&](auto tc) {
    constexpr int t = decltype(tc)::value;
    top(std::integral_constant<int, 15 + t>{});
    constexpr unsigned sl = SCH.slot[15 + t] * SLOT_BYTES;
    bf16x8 pf[2][2];
    pf[0][0] = pown[0]; pf[0][1] = pown[1];
    pf[1][0] = lds_b128(pbuf + (t & 1) * 8192 + (wid ^ 2) * 2048 + lane * 16);
    pf[1][1] = lds_b128(pbuf + (t & 1) * 8192 + (wid ^ 2) * 2048 + 1024 + lane * 16);
    if constexpr (t < 7) make_p(S[t + 1], (t + 1) & 1);
    else {
      float l2 = swap32_add(lsum);
      *(LAS float*)(xbuf + wid * 256 + lane * 4) = l2;
      lsum = l2;
    }
#pragma unroll
    for (int u = 0; u < 2; ++u) {
      const unsigned vr = lds0 + sl + (32 * (role ^ u) + 4 * hi + q4) * 512;
#pragma unroll
      for (int sp = 0; sp < 2; ++sp)
#pragma unroll
        for (int blk = 0; blk < 4; ++blk) {
          bf16x8 a = tr_frag(vr + trc[blk] + (16 * sp) * 512, vr + trc[blk] + (16 * sp + 8) * 512);
          o[blk] = __builtin_amdgcn_mfma_f32_32x32x16_bf16(a, pf[u][sp], o[blk], 0, 0, 0);
        }
    }
  });

  bf16x8 ofo[4][2], ofp[4][2];
  const unsigned obuf = lds0 + SCR + 0;
  f32x16 acc2[7];
#pragma unroll
  for (int i = 0; i < 7; ++i) acc2[i] = f32x16{};
  lds_sync();
  {
    float lt = lsum + *(LAS const float*)(xbuf + (wid ^ 2) * 256 + lane * 4);
    float inv = 1.0f / lt;
#pragma unroll
    for (int blk = 0; blk < 4; ++blk)
#pragma unroll
      for (int sp = 0; sp < 2; ++sp) {
        u32x4 w = {pk2(o[blk][8 * sp] * inv, o[blk][8 * sp + 1] * inv), pk2(o[blk][8 * sp + 2] * inv, o[blk][8 * sp + 3] * inv),
                   pk2(o[blk][8 * sp + 4] * inv, o[blk][8 * sp + 5] * inv), pk2(o[blk][8 * sp + 6] * inv, o[blk][8 * sp + 7] * inv)};
        ofo[blk][sp] = __builtin_bit_cast(bf16x8, w);
      }
  }
  lds_sync();
#pragma unroll
  for (int blk = 0; blk < 4; ++blk)
#pragma unroll
    for (int sp = 0; sp < 2; ++sp)
      *(LAS u32x4*)(obuf + wid * 8192 + (blk * 2 + sp) * 1024 + lane * 16) = __builtin_bit_cast(u32x4, ofo[blk][sp]);

  unsigned trw[7];
#pragma unroll
  for (int nb = 0; nb < 7; ++nb) trw[nb] = ((4 * ((7 * role + nb) ^ q4) + 2 * bb + (p >> 1)) << 4) + 8 * (p & 1);
  static_for<0, 8>([&](auto tc) {
    constexpr int t = decltype(tc)::value;
    top(std::integral_constant<int, 23 + t>{});
    if constexpr (t == 0) {
#pragma unroll
      for (int blk = 0; blk < 4; ++blk)
#pragma unroll
        for (int sp = 0; sp < 2; ++sp)
          ofp[blk][sp] = lds_b128(obuf + (wid ^ 2) * 8192 + (blk * 2 + sp) * 1024 + lane * 16);
    }
    constexpr unsigned sl = SCH.slot[23 + t] * SLOT_BYTES;
    const unsigned wr = lds0 + sl + (4 * hi + q4) * 1024;
#pragma unroll
    for (int sp = 0; sp < 2; ++sp) {
      bf16x8 bfr = ((t >> 2) == role) ? ofo[t & 3][sp] : ofp[t & 3][sp];
#pragma unroll
      for (int nb = 0; nb < 7; ++nb) {
        bf16x8 a = tr_frag(wr + trw[nb] + (16 * sp) * 1024, wr + trw[nb] + (16 * sp + 8) * 1024);
        acc2[nb] = __builtin_amdgcn_mfma_f32_32x32x16_bf16(a, bfr, acc2[nb], 0, 0, 0);
      }
    }
  });

  char* sp_ = slab + ((size_t)(((h * 8 + b) * 2 + qh) * 4 + wid) * 14) * 1024 + lane * 16;
#pragma unroll
  for (int nb = 0; nb < 7; ++nb)
#pragma unroll
    for (int g2 = 0; g2 < 2; ++g2) {
      u32x4 w = {pk2(acc2[nb][8 * g2], acc2[nb][8 * g2 + 1]), pk2(acc2[nb][8 * g2 + 2], acc2[nb][8 * g2 + 3]),
                 pk2(acc2[nb][8 * g2 + 4], acc2[nb][8 * g2 + 5]), pk2(acc2[nb][8 * g2 + 6], acc2[nb][8 * g2 + 7])};
      *(u32x4*)(sp_ + (nb * 2 + g2) * 1024) = w;
    }
}

__global__ __launch_bounds__(256) void reduce_kernel(const u32x4* __restrict__ slab, char* __restrict__ out) {
  const int i = blockIdx.x * 256 + threadIdx.x;
  float s[8];
#pragma unroll
  for (int k = 0; k < 8; ++k) s[k] = 0.f;
  u32x4 v[16];
#pragma unroll
  for (int h = 0; h < 16; ++h) v[h] = slab[(size_t)h * 57344 + i];
#pragma unroll
  for (int h = 0; h < 16; ++h)
#pragma unroll
    for (int k = 0; k < 4; ++k) {
      s[2 * k] += __uint_as_float(v[h][k] << 16);
      s[2 * k + 1] += __uint_as_float(v[h][k] & 0xffff0000u);
    }
  const int lane = i & 63, g2 = (i >> 6) & 1;
  int rest = i >> 7;
  const int nb = rest % 7; rest /= 7;
  const int wid = rest & 3, qh = (rest >> 2) & 1, b = rest >> 3;
  const int row = b * 128 + qh * 64 + (wid & 1) * 32 + (lane & 31);
  const int n0 = 32 * (7 * (wid >> 1) + nb) + 16 * g2 + 4 * (lane >> 5);
  char* o = out + ((size_t)row * 448 + n0) * 2;
  u32x2 w0 = {pk2(s[0], s[1]), pk2(s[2], s[3])};
  u32x2 w1 = {pk2(s[4], s[5]), pk2(s[6], s[7])};
  *(u32x2*)o = w0;
  *(u32x2*)(o + 16) = w1;
}

extern "C" void kernel_launch(void* const* d_in, const int* in_sizes, int n_in, void* d_out, int out_size,
                              void* d_ws, size_t ws_size, hipStream_t stream) {
  const char* x = (const char*)d_in[0];
  const char* k = (const char*)d_in[1];
  const char* v = (const char*)d_in[2];
  const char* wq = (const char*)d_in[3];
  const char* wo = (const char*)d_in[4];
  char* slab = (char*)d_ws;
  attn_kernel<<<256, 256, 0, stream>>>(x, k, v, wq, wo, slab);
  reduce_kernel<<<224, 256, 0, stream>>>((const u32x4*)slab, (char*)d_out);
}
```

```cpp
#include <hip/hip_runtime.h>
#include <hip/hip_bf16.h>
#include <stdint.h>
#include <type_traits>

typedef __bf16 bf16x8 __attribute__((ext_vector_type(8)));
typedef __bf16 bf16x2 __attribute__((ext_vector_type(2)));
typedef short s16x4 __attribute__((ext_vector_type(4)));
typedef short s16x8 __attribute__((ext_vector_type(8)));
typedef float f32x2 __attribute__((ext_vector_type(2)));
typedef float f32x16 __attribute__((ext_vector_type(16)));
typedef unsigned u32x2 __attribute__((ext_vector_type(2)));
typedef unsigned u32x4 __attribute__((ext_vector_type(4)));
#define LAS __attribute__((address_space(3)))
#define DI __device__ __forceinline__

constexpr int NSTEP = 31;
constexpr int NSLOT = 4;
constexpr unsigned SLOT_BYTES = 32768;
constexpr unsigned SCR = NSLOT * SLOT_BYTES;

struct Sched { int slot[NSTEP]; int wait[NSTEP]; int issue_at[NSTEP]; int pro[NSLOT]; };
constexpr Sched make_sched() {
  Sched s{};
  int ord[NSTEP] = {};
  for (int i = 0; i < NSTEP; ++i) ord[i] = i;
  int pos[NSTEP] = {};
  for (int i = 0; i < NSTEP; ++i) pos[ord[i]] = i;
  for (int i = 0; i < NSLOT; ++i) { s.pro[i] = ord[i]; s.slot[ord[i]] = i; }
  for (int t = 0; t < NSTEP; ++t) {
    int last = (t == 0) ? (NSLOT - 1) : (t + NSLOT - 2 < NSTEP - 1 ? t + NSLOT - 2 : NSTEP - 1);
    s.wait[t] = 8 * (last - pos[t]);
    s.issue_at[t] = -1;
    if (t >= 1 && t + NSLOT - 1 < NSTEP) { int e = ord[t + NSLOT - 1]; s.issue_at[t] = e; s.slot[e] = s.slot[t - 1]; }
  }
  return s;
}
constexpr Sched SCH = make_sched();

template <int B, int E, class F> DI void static_for(F&& f) {
  if constexpr (B < E) { f(std::integral_constant<int, B>{}); static_for<B + 1, E>(f); }
}

DI void dma16(unsigned lds_addr, const char* sbase, unsigned voff) {
  asm volatile("s_mov_b32 m0, %0\n\ts_nop 0\n\tglobal_load_lds_dwordx4 %1, %2"
               :: "s"(lds_addr), "v"(voff), "s"(sbase) : "memory");
}
template <int N> DI void top_sync() {
  asm volatile("s_waitcnt vmcnt(%0) lgkmcnt(0)\n\ts_barrier" :: "n"(N) : "memory");
}
DI void lds_sync() { asm volatile("s_waitcnt lgkmcnt(0)\n\ts_barrier" ::: "memory"); }

DI unsigned pk2(float a, float b) {
  f32x2 v = {a, b};
  bf16x2 r = __builtin_convertvector(v, bf16x2);
  return __builtin_bit_cast(unsigned, r);
}
DI bf16x8 tr_frag(unsigned a0, unsigned a1) {
  s16x4 lo = __builtin_amdgcn_ds_read_tr16_b64_v4i16((LAS s16x4*)a0);
  s16x4 hi = __builtin_amdgcn_ds_read_tr16_b64_v4i16((LAS s16x4*)a1);
  s16x8 r = __builtin_shufflevector(lo, hi, 0, 1, 2, 3, 4, 5, 6, 7);
  return __builtin_bit_cast(bf16x8, r);
}
DI bf16x8 lds_b128(unsigned a) { return *(LAS const bf16x8*)a; }
DI float swap32_max(float v) {
  auto rr = __builtin_amdgcn_permlane32_swap(__float_as_uint(v), __float_as_uint(v), false, false);
  return fmaxf(__uint_as_float(rr[0]), __uint_as_float(rr[1]));
}
DI float swap32_add(float v) {
  auto rr = __builtin_amdgcn_permlane32_swap(__float_as_uint(v), __float_as_uint(v), false, false);
  return __uint_as_float(rr[0]) + __uint_as_float(rr[1]);
}

__global__ __launch_bounds__(512, 2) void attn_kernel(const char* __restrict__ x, const char* __restrict__ Kc,
                                                      const char* __restrict__ Vc, const char* __restrict__ wq,
                                                      const char* __restrict__ wo, char* __restrict__ slab) {
  __shared__ __attribute__((aligned(1024))) char smem[163840];
  const unsigned lds0 = (unsigned)(size_t)smem;
  const int tid = threadIdx.x, lane = tid & 63;
  const int wid8 = __builtin_amdgcn_readfirstlane(tid >> 6);
  const int wid = wid8 & 3;
  const int qb = wid & 1, role = wid >> 1;
  const int hi = lane >> 5, l31 = lane & 31;
  const int bid = blockIdx.x;
  const int xcd = bid & 7, idx = bid >> 3;
  const int qh = idx & 1, hl = (idx >> 1) & 1, b = idx >> 2, h = xcd * 2 + hl;

  if (wid8 >= 4) {
    const char* Kb = Kc + (size_t)(b * 16 + h) * (512 * 512) + (size_t)((wid >> 1) * 256 + (wid & 1) * 16) * 512;
    const char* Vb = Vc + (size_t)(b * 16 + h) * (512 * 512) + (size_t)((wid >> 1) * 256 + (wid & 1) * 16) * 512;
    const char* Wqb = wq + (size_t)(wid * 16) * 8192 + h * 512;
    const char* Wob = wo + (size_t)(h * 256 + wid * 8) * 896;
    const unsigned ldsw = lds0 + wid * 8192;
    auto issue = [&](auto sc) {
      constexpr int S = decltype(sc)::value;
      constexpr unsigned sl = SCH.slot[S] * SLOT_BYTES;
      static_for<0, 8>([&](auto jc) {
        constexpr int j = decltype(jc)::value;
        if constexpr (S < 7) {
          unsigned voff = hi * 8192 + ((l31 ^ ((2 * (j & 1) + hi) << 2)) << 4);
          dma16(ldsw + sl + j * 1024, Wqb + (size_t)(64 * S + 2 * j) * 8192, voff);
        } else if constexpr (S < 15) {
          unsigned voff = hi * 512 + ((l31 ^ hi ^ (2 * j)) << 4);
          dma16(ldsw + sl + j * 1024, Kb + (size_t)(32 * (S - 7) + 2 * j) * 512, voff);
        } else if constexpr (S < 23) {
          unsigned voff = hi * 512 + ((l31 ^ ((2 * (j & 1) + hi) << 2)) << 4);
          dma16(ldsw + sl + j * 1024, Vb + (size_t)(32 * (S - 15) + 2 * j) * 512, voff);
        } else {
          unsigned g = lane ^ ((j & 3) << 2);
          g = (g >= 56) ? g - 8 : g;
          dma16(ldsw + sl + j * 1024, Wob + (size_t)(32 * (S - 23) + j) * 896, g << 4);
        }
      });
    };
    static_for<0, NSLOT>([&](auto ic) { issue(std::integral_constant<int, SCH.pro[decltype(ic)::value]>{}); });
    static_for<0, NSTEP>([&](auto sc) {
      constexpr int S = decltype(sc)::value;
      top_sync<SCH.wait[S]>();
      if constexpr (SCH.issue_at[S] >= 0) issue(std::integral_constant<int, SCH.issue_at[S]>{});
      if constexpr (S == 14) { asm volatile("s_barrier" ::: "memory"); }
      if constexpr (S == 22) { asm volatile("s_barrier" ::: "memory"); asm volatile("s_barrier" ::: "memory"); }
    });
    return;
  }

  bf16x8 xf[28];
  {
    const char* xrow = x + ((size_t)(b * 128 + qh * 64 + qb * 32 + l31) * 448 + 8 * hi) * 2;
#pragma unroll
    for (int s = 0; s < 28; ++s) xf[s] = *(const bf16x8*)(xrow + 32 * s);
  }
  auto top = [&](auto sc) { lds_sync(); };

  const int q4 = (lane & 15) >> 2, p = lane & 3, bb = (lane >> 4) & 1;
  unsigned trc[4];
#pragma unroll
  for (int blk = 0; blk < 4; ++blk) trc[blk] = ((16 * role + 4 * (blk ^ q4) + 2 * bb + (p >> 1)) << 4) + 8 * (p & 1);

  f32x16 acc[4];
#pragma unroll
  for (int i = 0; i < 4; ++i) acc[i] = f32x16{};
  static_for<0, 7>([&](auto tc) {
    constexpr int t = decltype(tc)::value;
    top(std::integral_constant<int, t>{});
    constexpr unsigned sl = SCH.slot[t] * SLOT_BYTES;
    const unsigned rb = lds0 + sl + (8 * hi + q4) * 512;
#pragma unroll
    for (int ks = 0; ks < 4; ++ks)
#pragma unroll
      for (int blk = 0; blk < 4; ++blk) {
        bf16x8 a = tr_frag(rb + trc[blk] + (16 * ks) * 512, rb + trc[blk] + (16 * ks + 4) * 512);
        acc[blk] = __builtin_amdgcn_mfma_f32_32x32x16_bf16(a, xf[4 * t + ks], acc[blk], 0, 0, 0);
      }
  });
  const unsigned qrow = lds0 + SCR + (32 * qb + l31) * 512;
#pragma unroll
  for (int blk = 0; blk < 4; ++blk)
#pragma unroll
    for (int g = 0; g < 4; ++g) {
      u32x2 w = {pk2(acc[blk][4 * g], acc[blk][4 * g + 1]), pk2(acc[blk][4 * g + 2], acc[blk][4 * g + 3])};
      *(LAS u32x2*)(qrow + (((16 * role + 4 * blk + g) ^ (l31 & 15)) << 4) + 8 * hi) = w;
    }

  f32x16 S[8];
  bf16x8 qf[16];
  float mx = -3.0e38f;
  static_for<0, 8>([&](auto tc) {
    constexpr int t = decltype(tc)::value;
    top(std::integral_constant<int, 7 + t>{});
    if constexpr (t == 0) {
#pragma unroll
      for (int s = 0; s < 16; ++s) qf[s] = lds_b128(qrow + (((2 * s + hi) ^ (l31 & 15)) << 4));
    }
    constexpr unsigned sl = SCH.slot[7 + t] * SLOT_BYTES;
    const unsigned kr = lds0 + sl + (32 * role + l31) * 512;
    f32x16 c = f32x16{};
#pragma unroll
    for (int s = 0; s < 16; ++s) {
      bf16x8 a = lds_b128(kr + (((2 * s + hi) ^ (l31 & 15)) << 4));
      c = __builtin_amdgcn_mfma_f32_32x32x16_bf16(a, qf[s], c, 0, 0, 0);
    }
    S[t] = c;
#pragma unroll
    for (int r = 0; r < 16; ++r) mx = fmaxf(mx, c[r]);
  });
  mx = swap32_max(mx);
  const unsigned xbuf = lds0 + SCR + 16384;
  *(LAS float*)(xbuf + wid * 256 + lane * 4) = mx;
  lds_sync();
  mx = fmaxf(mx, *(LAS const float*)(xbuf + (wid ^ 2) * 256 + lane * 4));

  constexpr float C = 0.0625f * 1.44269504088896340736f;
  const float mC = -mx * C;
  float lsum = 0.f;
  const unsigned pbuf = lds0 + SCR;
  bf16x8 pown[2];
  auto make_p = [&](const f32x16& s, int par) {
    float pe[16];
#pragma unroll
    for (int r = 0; r < 16; ++r) { pe[r] = __builtin_amdgcn_exp2f(fmaf(s[r], C, mC)); lsum += pe[r]; }
#pragma unroll
    for (int sp = 0; sp < 2; ++sp) {
      u32x4 w = {pk2(pe[8 * sp], pe[8 * sp + 1]), pk2(pe[8 * sp + 2], pe[8 * sp + 3]),
                 pk2(pe[8 * sp + 4], pe[8 * sp + 5]), pk2(pe[8 * sp + 6], pe[8 * sp + 7])};
      pown[sp] = __builtin_bit_cast(bf16x8, w);
      *(LAS u32x4*)(pbuf + par * 8192 + wid * 2048 + sp * 1024 + lane * 16) = w;
    }
  };
  make_p(S[0], 0);

  f32x16 o[4];
#pragma unroll
  for (int i = 0; i < 4; ++i) o[i] = f32x16{};
  static_for<0, 8>([&](auto tc) {
    constexpr int t = decltype(tc)::value;
    top(std::integral_constant<int, 15 + t>{});
    constexpr unsigned sl = SCH.slot[15 + t] * SLOT_BYTES;
    bf16x8 pf[2][2];
    pf[0][0] = pown[0]; pf[0][1] = pown[1];
    pf[1][0] = lds_b128(pbuf + (t & 1) * 8192 + (wid ^ 2) * 2048 + lane * 16);
    pf[1][1] = lds_b128(pbuf + (t & 1) * 8192 + (wid ^ 2) * 2048 + 1024 + lane * 16);
    if constexpr (t < 7) make_p(S[t + 1], (t + 1) & 1);
    else {
      float l2 = swap32_add(lsum);
      *(LAS float*)(xbuf + wid * 256 + lane * 4) = l2;
      lsum = l2;
    }
#pragma unroll
    for (int u = 0; u < 2; ++u) {
      const unsigned vr = lds0 + sl + (32 * (role ^ u) + 4 * hi + q4) * 512;
#pragma unroll
      for (int sp = 0; sp < 2; ++sp)
#pragma unroll
        for (int blk = 0; blk < 4; ++blk) {
          bf16x8 a = tr_frag(vr + trc[blk] + (16 * sp) * 512, vr + trc[blk] + (16 * sp + 8) * 512);
          o[blk] = __builtin_amdgcn_mfma_f32_32x32x16_bf16(a, pf[u][sp], o[blk], 0, 0, 0);
        }
    }
  });

  bf16x8 ofo[4][2], ofp[4][2];
  const unsigned obuf = lds0 + SCR + 0;
  f32x16 acc2[7];
#pragma unroll
  for (int i = 0; i < 7; ++i) acc2[i] = f32x16{};
  lds_sync();
  {
    float lt = lsum + *(LAS const float*)(xbuf + (wid ^ 2) * 256 + lane * 4);
    float inv = 1.0f / lt;
#pragma unroll
    for (int blk = 0; blk < 4; ++blk)
#pragma unroll
      for (int sp = 0; sp < 2; ++sp) {
        u32x4 w = {pk2(o[blk][8 * sp] * inv, o[blk][8 * sp + 1] * inv), pk2(o[blk][8 * sp + 2] * inv, o[blk][8 * sp + 3] * inv),
                   pk2(o[blk][8 * sp + 4] * inv, o[blk][8 * sp + 5] * inv), pk2(o[blk][8 * sp + 6] * inv, o[blk][8 * sp + 7] * inv)};
        ofo[blk][sp] = __builtin_bit_cast(bf16x8, w);
      }
  }
  lds_sync();
#pragma unroll
  for (int blk = 0; blk < 4; ++blk)
#pragma unroll
    for (int sp = 0; sp < 2; ++sp)
      *(LAS u32x4*)(obuf + wid * 8192 + (blk * 2 + sp) * 1024 + lane * 16) = __builtin_bit_cast(u32x4, ofo[blk][sp]);

  unsigned trw[7];
#pragma unroll
  for (int nb = 0; nb < 7; ++nb) trw[nb] = ((4 * ((7 * role + nb) ^ q4) + 2 * bb + (p >> 1)) << 4) + 8 * (p & 1);
  static_for<0, 8>([&](auto tc) {
    constexpr int t = decltype(tc)::value;
    top(std::integral_constant<int, 23 + t>{});
    if constexpr (t == 0) {
#pragma unroll
      for (int blk = 0; blk < 4; ++blk)
#pragma unroll
        for (int sp = 0; sp < 2; ++sp)
          ofp[blk][sp] = lds_b128(obuf + (wid ^ 2) * 8192 + (blk * 2 + sp) * 1024 + lane * 16);
    }
    constexpr unsigned sl = SCH.slot[23 + t] * SLOT_BYTES;
    const unsigned wr = lds0 + sl + (4 * hi + q4) * 1024;
#pragma unroll
    for (int sp = 0; sp < 2; ++sp) {
      bf16x8 bfr = ((t >> 2) == role) ? ofo[t & 3][sp] : ofp[t & 3][sp];
#pragma unroll
      for (int nb = 0; nb < 7; ++nb) {
        bf16x8 a = tr_frag(wr + trw[nb] + (16 * sp) * 1024, wr + trw[nb] + (16 * sp + 8) * 1024);
        acc2[nb] = __builtin_amdgcn_mfma_f32_32x32x16_bf16(a, bfr, acc2[nb], 0, 0, 0);
      }
    }
  });

  char* sp_ = slab + ((size_t)(((h * 8 + b) * 2 + qh) * 4 + wid) * 14) * 1024 + lane * 16;
#pragma unroll
  for (int nb = 0; nb < 7; ++nb)
#pragma unroll
    for (int g2 = 0; g2 < 2; ++g2) {
      u32x4 w = {pk2(acc2[nb][8 * g2], acc2[nb][8 * g2 + 1]), pk2(acc2[nb][8 * g2 + 2], acc2[nb][8 * g2 + 3]),
                 pk2(acc2[nb][8 * g2 + 4], acc2[nb][8 * g2 + 5]), pk2(acc2[nb][8 * g2 + 6], acc2[nb][8 * g2 + 7])};
      *(u32x4*)(sp_ + (nb * 2 + g2) * 1024) = w;
    }
}

__global__ __launch_bounds__(256) void reduce_kernel(const u32x4* __restrict__ slab, char* __restrict__ out) {
  const int i = blockIdx.x * 256 + threadIdx.x;
  float s[8];
#pragma unroll
  for (int k = 0; k < 8; ++k) s[k] = 0.f;
  u32x4 v[16];
#pragma unroll
  for (int h = 0; h < 16; ++h) v[h] = slab[(size_t)h * 57344 + i];
#pragma unroll
  for (int h = 0; h < 16; ++h)
#pragma unroll
    for (int k = 0; k < 4; ++k) {
      s[2 * k] += __uint_as_float(v[h][k] << 16);
      s[2 * k + 1] += __uint_as_float(v[h][k] & 0xffff0000u);
    }
  const int lane = i & 63, g2 = (i >> 6) & 1;
  int rest = i >> 7;
  const int nb = rest % 7; rest /= 7;
  const int wid = rest & 3, qh = (rest >> 2) & 1, b = rest >> 3;
  const int row = b * 128 + qh * 64 + (wid & 1) * 32 + (lane & 31);
  const int n0 = 32 * (7 * (wid >> 1) + nb) + 16 * g2 + 4 * (lane >> 5);
  char* o = out + ((size_t)row * 448 + n0) * 2;
  u32x2 w0 = {pk2(s[0], s[1]), pk2(s[2], s[3])};
  u32x2 w1 = {pk2(s[4], s[5]), pk2(s[6], s[7])};
  *(u32x2*)o = w0;
  *(u32x2*)(o + 16) = w1;
}

extern "C" void kernel_launch(void* const* d_in, const int* in_sizes, int n_in, void* d_out, int out_size,
                              void* d_ws, size_t ws_size, hipStream_t stream) {
  const char* x = (const char*)d_in[0];
  const char* k = (const char*)d_in[1];
  const char* v = (const char*)d_in[2];
  const char* wq = (const char*)d_in[3];
  const char* wo = (const char*)d_in[4];
  char* slab = (char*)d_ws;
  attn_kernel<<<256, 512, 0, stream>>>(x, k, v, wq, wo, slab);
  reduce_kernel<<<224, 256, 0, stream>>>((const u32x4*)slab, (char*)d_out);
}
```

```cpp
#include <hip/hip_runtime.h>
#include <hip/hip_bf16.h>
#include <stdint.h>
#include <type_traits>

typedef __bf16 bf16x8 __attribute__((ext_vector_type(8)));
typedef __bf16 bf16x2 __attribute__((ext_vector_type(2)));
typedef short s16x4 __attribute__((ext_vector_type(4)));
typedef short s16x8 __attribute__((ext_vector_type(8)));
typedef float f32x2 __attribute__((ext_vector_type(2)));
typedef float f32x16 __attribute__((ext_vector_type(16)));
typedef unsigned u32x2 __attribute__((ext_vector_type(2)));
typedef unsigned u32x4 __attribute__((ext_vector_type(4)));
#define LAS __attribute__((address_space(3)))
#define DI __device__ __forceinline__

constexpr int NSTEP = 31;
constexpr int NSLOT = 4;
constexpr unsigned SLOT_BYTES = 32768;
constexpr unsigned SCR = NSLOT * SLOT_BYTES;

struct Sched { int slot[NSTEP]; int wait[NSTEP]; int issue_at[NSTEP]; };
constexpr int npieces(int t) { return t < 7 ? 10 : 8; }
constexpr Sched make_sched() {
  Sched s{};
  for (int t = 0; t < NSTEP; ++t) {
    s.slot[t] = t % NSLOT;
    int last = (t == 0) ? (NSLOT - 1) : (t + NSLOT - 2 < NSTEP - 1 ? t + NSLOT - 2 : NSTEP - 1);
    int w = 0;
    for (int u = t + 1; u <= last; ++u) w += npieces(u);
    s.wait[t] = w;
    s.issue_at[t] = (t >= 1 && t + NSLOT - 1 < NSTEP) ? t + NSLOT - 1 : -1;
  }
  return s;
}
constexpr Sched SCH = make_sched();

template <int B, int E, class F> DI void static_for(F&& f) {
  if constexpr (B < E) { f(std::integral_constant<int, B>{}); static_for<B + 1, E>(f); }
}

template <int N, int D, class L, class M> DI void pipe(L&& load, M&& mma) {
  bf16x8 fr[N];
  static_for<0, D>([&](auto ic) { fr[decltype(ic)::value] = load(ic); });
  __builtin_amdgcn_sched_barrier(0);
  static_for<0, N>([&](auto ic) {
    constexpr int i = decltype(ic)::value;
    if constexpr (i + D < N) fr[i + D] = load(std::integral_constant<int, i + D>{});
    mma(ic, fr[i]);
    __builtin_amdgcn_sched_barrier(0);
  });
}

DI void dma16(unsigned lds_addr, const char* sbase, unsigned voff) {
  asm volatile("s_mov_b32 m0, %0\n\ts_nop 0\n\tglobal_load_lds_dwordx4 %1, %2"
               :: "s"(lds_addr), "v"(voff), "s"(sbase) : "memory");
}
template <int N> DI void top_sync() {
  asm volatile("s_waitcnt vmcnt(%0) lgkmcnt(0)\n\ts_barrier" :: "n"(N) : "memory");
}
DI void lds_sync() { asm volatile("s_waitcnt lgkmcnt(0)\n\ts_barrier" ::: "memory"); }

DI unsigned pk2(float a, float b) {
  f32x2 v = {a, b};
  bf16x2 r = __builtin_convertvector(v, bf16x2);
  return __builtin_bit_cast(unsigned, r);
}
DI bf16x8 tr_frag(unsigned a0, unsigned a1) {
  s16x4 lo = __builtin_amdgcn_ds_read_tr16_b64_v4i16((LAS s16x4*)a0);
  s16x4 hi = __builtin_amdgcn_ds_read_tr16_b64_v4i16((LAS s16x4*)a1);
  s16x8 r = __builtin_shufflevector(lo, hi, 0, 1, 2, 3, 4, 5, 6, 7);
  return __builtin_bit_cast(bf16x8, r);
}
DI bf16x8 lds_b128(unsigned a) { return *(LAS const bf16x8*)a; }
DI float swap32_max(float v) {
  auto rr = __builtin_amdgcn_permlane32_swap(__float_as_uint(v), __float_as_uint(v), false, false);
  return fmaxf(__uint_as_float(rr[0]), __uint_as_float(rr[1]));
}
DI float swap32_add(float v) {
  auto rr = __builtin_amdgcn_permlane32_swap(__float_as_uint(v), __float_as_uint(v), false, false);
  return __uint_as_float(rr[0]) + __uint_as_float(rr[1]);
}

__global__ __launch_bounds__(512, 2) void attn_kernel(const char* __restrict__ x, const char* __restrict__ Kc,
                                                      const char* __restrict__ Vc, const char* __restrict__ wq,
                                                      const char* __restrict__ wo, char* __restrict__ slab) {
  __shared__ __attribute__((aligned(1024))) char smem[163840];
  const unsigned lds0 = (unsigned)(size_t)smem;
  const int tid = threadIdx.x, lane = tid & 63;
  const int wid8 = __builtin_amdgcn_readfirstlane(tid >> 6);
  const int wid = wid8 & 3;
  const int qb = wid & 1, role = wid >> 1;
  const int hi = lane >> 5, l31 = lane & 31;
  const int bid = blockIdx.x;
  const int xcd = bid & 7, idx = bid >> 3;
  const int qh = idx & 1, hl = (idx >> 1) & 1, b = idx >> 2, h = xcd * 2 + hl;

  if (wid8 >= 4) {
    const size_t kvo = (size_t)(b * 16 + h) * (512 * 512) + (size_t)((wid >> 1) * 256 + (wid & 1) * 16) * 512;
    const char* KbA = Kc + kvo + (size_t)qh * (128 * 512);
    const char* KbB = Kc + kvo - (size_t)qh * (128 * 512);
    const char* VbA = Vc + kvo + (size_t)qh * (128 * 512);
    const char* VbB = Vc + kvo - (size_t)qh * (128 * 512);
    const char* Wqb = wq + (size_t)(wid * 16) * 8192 + h * 512;
    const char* Wob = wo + (size_t)(h * 256 + wid * 8) * 896;
    const char* Xb = x + (size_t)(b * 128 + qh * 64 + wid * 16) * 896;
    const unsigned ldsw = lds0 + wid * 8192;
    auto issue = [&](auto sc) {
      constexpr int S = decltype(sc)::value;
      constexpr unsigned sl = SCH.slot[S] * SLOT_BYTES;
      if constexpr (S < 7) {
        static_for<0, 2>([&](auto jc) {
          constexpr int j = decltype(jc)::value;
          unsigned voff = (lane >> 3) * 896 + (((lane & 7) ^ (lane >> 3) ^ j) << 4);
          dma16(lds0 + SCR + SCH.slot[S] * 8192 + (2 * wid + j) * 1024, Xb + (size_t)(8 * j) * 896 + 128 * S, voff);
        });
      }
      static_for<0, 8>([&](auto jc) {
        constexpr int j = decltype(jc)::value;
        if constexpr (S < 7) {
          unsigned voff = hi * 8192 + ((l31 ^ ((2 * (j & 1) + hi) << 2)) << 4);
          dma16(ldsw + sl + j * 1024, Wqb + (size_t)(64 * S + 2 * j) * 8192, voff);
        } else if constexpr (S < 15) {
          unsigned voff = hi * 512 + ((l31 ^ hi ^ (2 * j)) << 4);
          dma16(ldsw + sl + j * 1024, (S - 7 < 4 ? KbA : KbB) + (size_t)(32 * (S - 7) + 2 * j) * 512, voff);
        } else if constexpr (S < 23) {
          unsigned voff = hi * 512 + ((l31 ^ ((2 * (j & 1) + hi) << 2)) << 4);
          dma16(ldsw + sl + j * 1024, (S - 15 < 4 ? VbA : VbB) + (size_t)(32 * (S - 15) + 2 * j) * 512, voff);
        } else {
          unsigned g = lane ^ ((j & 3) << 2);
          g = (g >= 56) ? g - 8 : g;
          dma16(ldsw + sl + j * 1024, Wob + (size_t)(32 * (S - 23) + j) * 896, g << 4);
        }
      });
    };
    static_for<0, NSLOT>([&](auto ic) { issue(ic); });
    static_for<0, NSTEP>([&](auto sc) {
      constexpr int S = decltype(sc)::value;
      top_sync<SCH.wait[S]>();
      if constexpr (SCH.issue_at[S] >= 0) issue(std::integral_constant<int, SCH.issue_at[S]>{});
      if constexpr (S == 6 || S == 14) { asm volatile("s_barrier" ::: "memory"); }
      if constexpr (S == 22) { asm volatile("s_barrier" ::: "memory"); asm volatile("s_barrier" ::: "memory"); }
    });
    return;
  }

  auto top = [&](auto sc) { lds_sync(); };

  const int q4 = (lane & 15) >> 2, p = lane & 3, bb = (lane >> 4) & 1;
  unsigned trc[4];
#pragma unroll
  for (int blk = 0; blk < 4; ++blk) trc[blk] = ((16 * role + 4 * (blk ^ q4) + 2 * bb + (p >> 1)) << 4) + 8 * (p & 1);

  f32x16 acc[4];
#pragma unroll
  for (int i = 0; i < 4; ++i) acc[i] = f32x16{};
  const unsigned xr = lds0 + SCR + (4 * qb + (l31 >> 3)) * 1024 + (l31 & 7) * 128;
  const unsigned xsw = (l31 & 7) ^ ((l31 >> 3) & 1) ^ hi;
  static_for<0, 7>([&](auto tc) {
    constexpr int t = decltype(tc)::value;
    top(tc);
    constexpr unsigned sl = SCH.slot[t] * SLOT_BYTES;
    const unsigned rb = lds0 + sl + (8 * hi + q4) * 512;
    bf16x8 xf[4];
#pragma unroll
    for (int ks = 0; ks < 4; ++ks) xf[ks] = lds_b128(xr + SCH.slot[t] * 8192 + (((2 * ks) ^ xsw) << 4));
    pipe<16, 4>(
        [&](auto ic) { constexpr int i = decltype(ic)::value; constexpr int ks = i >> 2, blk = i & 3;
                       return tr_frag(rb + trc[blk] + (16 * ks) * 512, rb + trc[blk] + (16 * ks + 4) * 512); },
        [&](auto ic, bf16x8 a) { constexpr int i = decltype(ic)::value; constexpr int ks = i >> 2, blk = i & 3;
                       acc[blk] = __builtin_amdgcn_mfma_f32_32x32x16_bf16(a, xf[ks], acc[blk], 0, 0, 0); });
  });
  lds_sync();
  const unsigned qrow = lds0 + SCR + (32 * qb + l31) * 512;
#pragma unroll
  for (int blk = 0; blk < 4; ++blk)
#pragma unroll
    for (int g = 0; g < 4; ++g) {
      u32x2 w = {pk2(acc[blk][4 * g], acc[blk][4 * g + 1]), pk2(acc[blk][4 * g + 2], acc[blk][4 * g + 3])};
      *(LAS u32x2*)(qrow + (((16 * role + 4 * blk + g) ^ (l31 & 15)) << 4) + 8 * hi) = w;
    }

  f32x16 S[8];
  bf16x8 qf[16];
  float mx = -3.0e38f;
  static_for<0, 8>([&](auto tc) {
    constexpr int t = decltype(tc)::value;
    top(std::integral_constant<int, 7 + t>{});
    if constexpr (t == 0) {
#pragma unroll
      for (int s = 0; s < 16; ++s) qf[s] = lds_b128(qrow + (((2 * s + hi) ^ (l31 & 15)) << 4));
    }
    constexpr unsigned sl = SCH.slot[7 + t] * SLOT_BYTES;
    const unsigned kr = lds0 + sl + (32 * role + l31) * 512;
    f32x16 c = f32x16{};
    pipe<16, 4>(
        [&](auto ic) { constexpr int s = decltype(ic)::value; return lds_b128(kr + ((((2 * (s & 7)) + hi) ^ (l31 & 15)) << 4) + (s >> 3) * 256); },
        [&](auto ic, bf16x8 a) { constexpr int s = decltype(ic)::value;
                       c = __builtin_amdgcn_mfma_f32_32x32x16_bf16(a, qf[s], c, 0, 0, 0); });
    S[t] = c;
#pragma unroll
    for (int r = 0; r < 16; ++r) mx = fmaxf(mx, c[r]);
  });
  mx = swap32_max(mx);
  const unsigned xbuf = lds0 + SCR + 16384;
  *(LAS float*)(xbuf + wid * 256 + lane * 4) = mx;
  lds_sync();
  mx = fmaxf(mx, *(LAS const float*)(xbuf + (wid ^ 2) * 256 + lane * 4));

  constexpr float C = 0.0625f * 1.44269504088896340736f;
  const float mC = -mx * C;
  float lsum = 0.f;
  const unsigned pbuf = lds0 + SCR;
  bf16x8 pown[2];
  auto make_p = [&](const f32x16& s, int par) {
    float pe[16];
#pragma unroll
    for (int r = 0; r < 16; ++r) { pe[r] = __builtin_amdgcn_exp2f(fmaf(s[r], C, mC)); lsum += pe[r]; }
#pragma unroll
    for (int sp = 0; sp < 2; ++sp) {
      u32x4 w = {pk2(pe[8 * sp], pe[8 * sp + 1]), pk2(pe[8 * sp + 2], pe[8 * sp + 3]),
                 pk2(pe[8 * sp + 4], pe[8 * sp + 5]), pk2(pe[8 * sp + 6], pe[8 * sp + 7])};
      pown[sp] = __builtin_bit_cast(bf16x8, w);
      *(LAS u32x4*)(pbuf + par * 8192 + wid * 2048 + sp * 1024 + lane * 16) = w;
    }
  };
  make_p(S[0], 0);

  f32x16 o[4];
#pragma unroll
  for (int i = 0; i < 4; ++i) o[i] = f32x16{};
  static_for<0, 8>([&](auto tc) {
    constexpr int t = decltype(tc)::value;
    top(std::integral_constant<int, 15 + t>{});
    constexpr unsigned sl = SCH.slot[15 + t] * SLOT_BYTES;
    bf16x8 pf[2][2];
    pf[0][0] = pown[0]; pf[0][1] = pown[1];
    pf[1][0] = lds_b128(pbuf + (t & 1) * 8192 + (wid ^ 2) * 2048 + lane * 16);
    pf[1][1] = lds_b128(pbuf + (t & 1) * 8192 + (wid ^ 2) * 2048 + 1024 + lane * 16);
    const unsigned vr0 = lds0 + sl + (32 * role + 4 * hi + q4) * 512;
    const unsigned vr1 = lds0 + sl + (32 * (role ^ 1) + 4 * hi + q4) * 512;
    float pe[16];
    unsigned pk[8];
    pipe<16, 4>(
        [&](auto ic) { constexpr int i = decltype(ic)::value; constexpr int u = i >> 3, sp = (i >> 2) & 1, blk = i & 3;
                       const unsigned vr = u ? vr1 : vr0;
                       return tr_frag(vr + trc[blk] + (16 * sp) * 512, vr + trc[blk] + (16 * sp + 8) * 512); },
        [&](auto ic, bf16x8 a) { constexpr int i = decltype(ic)::value; constexpr int u = i >> 3, sp = (i >> 2) & 1, blk = i & 3;
                       o[blk] = __builtin_amdgcn_mfma_f32_32x32x16_bf16(a, pf[u][sp], o[blk], 0, 0, 0);
                       if constexpr (t < 7) {
                         pe[i] = __builtin_amdgcn_exp2f(fmaf(S[t + 1][i], C, mC)); lsum += pe[i];
                         if constexpr (i & 1) pk[i >> 1] = pk2(pe[i - 1], pe[i]);
                         if constexpr ((i & 7) == 7) {
                           constexpr int s4 = (i >> 3) * 4;
                           u32x4 w = {pk[s4], pk[s4 + 1], pk[s4 + 2], pk[s4 + 3]};
                           pown[i >> 3] = __builtin_bit_cast(bf16x8, w);
                           *(LAS u32x4*)(pbuf + ((t + 1) & 1) * 8192 + wid * 2048 + (i >> 3) * 1024 + lane * 16) = w;
                         }
                       } });
    if constexpr (t == 7) {
      float l2 = swap32_add(lsum);
      *(LAS float*)(xbuf + wid * 256 + lane * 4) = l2;
      lsum = l2;
    }
  });

  bf16x8 ofo[4][2], ofp[4][2];
  const unsigned obuf = lds0 + SCR + 0;
  f32x16 acc2[7];
#pragma unroll
  for (int i = 0; i < 7; ++i) acc2[i] = f32x16{};
  lds_sync();
  {
    float lt = lsum + *(LAS const float*)(xbuf + (wid ^ 2) * 256 + lane * 4);
    float inv = 1.0f / lt;
#pragma unroll
    for (int blk = 0; blk < 4; ++blk)
#pragma unroll
      for (int sp = 0; sp < 2; ++sp) {
        u32x4 w = {pk2(o[blk][8 * sp] * inv, o[blk][8 * sp + 1] * inv), pk2(o[blk][8 * sp + 2] * inv, o[blk][8 * sp + 3] * inv),
                   pk2(o[blk][8 * sp + 4] * inv, o[blk][8 * sp + 5] * inv), pk2(o[blk][8 * sp + 6] * inv, o[blk][8 * sp + 7] * inv)};
        ofo[blk][sp] = __builtin_bit_cast(bf16x8, w);
      }
  }
  lds_sync();
#pragma unroll
  for (int blk = 0; blk < 4; ++blk)
#pragma unroll
    for (int sp = 0; sp < 2; ++sp)
      *(LAS u32x4*)(obuf + wid * 8192 + (blk * 2 + sp) * 1024 + lane * 16) = __builtin_bit_cast(u32x4, ofo[blk][sp]);

  unsigned trw[7];
#pragma unroll
  for (int nb = 0; nb < 7; ++nb) trw[nb] = ((4 * ((7 * role + nb) ^ q4) + 2 * bb + (p >> 1)) << 4) + 8 * (p & 1);
  static_for<0, 8>([&](auto tc) {
    constexpr int t = decltype(tc)::value;
    top(std::integral_constant<int, 23 + t>{});
    if constexpr (t == 0) {
#pragma unroll
      for (int blk = 0; blk < 4; ++blk)
#pragma unroll
        for (int sp = 0; sp < 2; ++sp)
          ofp[blk][sp] = lds_b128(obuf + (wid ^ 2) * 8192 + (blk * 2 + sp) * 1024 + lane * 16);
    }
    constexpr unsigned sl = SCH.slot[23 + t] * SLOT_BYTES;
    const unsigned wr = lds0 + sl + (4 * hi + q4) * 1024;
    const bf16x8 bf0 = ((t >> 2) == role) ? ofo[t & 3][0] : ofp[t & 3][0];
    const bf16x8 bf1 = ((t >> 2) == role) ? ofo[t & 3][1] : ofp[t & 3][1];
    pipe<14, 4>(
        [&](auto ic) { constexpr int i = decltype(ic)::value; constexpr int sp = i / 7, nb = i % 7;
                       return tr_frag(wr + trw[nb] + (16 * sp) * 1024, wr + trw[nb] + (16 * sp + 8) * 1024); },
        [&](auto ic, bf16x8 a) { constexpr int i = decltype(ic)::value; constexpr int sp = i / 7, nb = i % 7;
                       acc2[nb] = __builtin_amdgcn_mfma_f32_32x32x16_bf16(a, sp ? bf1 : bf0, acc2[nb], 0, 0, 0); });
  });

  char* sp_ = slab + ((size_t)(((h * 8 + b) * 2 + qh) * 4 + wid) * 14) * 1024 + lane * 16;
#pragma unroll
  for (int nb = 0; nb < 7; ++nb)
#pragma unroll
    for (int g2 = 0; g2 < 2; ++g2) {
      u32x4 w = {pk2(acc2[nb][8 * g2], acc2[nb][8 * g2 + 1]), pk2(acc2[nb][8 * g2 + 2], acc2[nb][8 * g2 + 3]),
                 pk2(acc2[nb][8 * g2 + 4], acc2[nb][8 * g2 + 5]), pk2(acc2[nb][8 * g2 + 6], acc2[nb][8 * g2 + 7])};
      *(u32x4*)(sp_ + (nb * 2 + g2) * 1024) = w;
    }
}

__global__ __launch_bounds__(256) void reduce_kernel(const u32x4* __restrict__ slab, char* __restrict__ out) {
  const int i = blockIdx.x * 256 + threadIdx.x;
  float s[8];
#pragma unroll
  for (int k = 0; k < 8; ++k) s[k] = 0.f;
  u32x4 v[16];
#pragma unroll
  for (int h = 0; h < 16; ++h) v[h] = slab[(size_t)h * 57344 + i];
#pragma unroll
  for (int h = 0; h < 16; ++h)
#pragma unroll
    for (int k = 0; k < 4; ++k) {
      s[2 * k] += __uint_as_float(v[h][k] << 16);
      s[2 * k + 1] += __uint_as_float(v[h][k] & 0xffff0000u);
    }
  const int lane = i & 63, g2 = (i >> 6) & 1;
  int rest = i >> 7;
  const int nb = rest % 7; rest /= 7;
  const int wid = rest & 3, qh = (rest >> 2) & 1, b = rest >> 3;
  const int row = b * 128 + qh * 64 + (wid & 1) * 32 + (lane & 31);
  const int n0 = 32 * (7 * (wid >> 1) + nb) + 16 * g2 + 4 * (lane >> 5);
  char* o = out + ((size_t)row * 448 + n0) * 2;
  u32x2 w0 = {pk2(s[0], s[1]), pk2(s[2], s[3])};
  u32x2 w1 = {pk2(s[4], s[5]), pk2(s[6], s[7])};
  *(u32x2*)o = w0;
  *(u32x2*)(o + 16) = w1;
}

extern "C" void kernel_launch(void* const* d_in, const int* in_sizes, int n_in, void* d_out, int out_size,
                              void* d_ws, size_t ws_size, hipStream_t stream) {
  const char* x = (const char*)d_in[0];
  const char* k = (const char*)d_in[1];
  const char* v = (const char*)d_in[2];
  const char* wq = (const char*)d_in[3];
  const char* wo = (const char*)d_in[4];
  char* slab = (char*)d_ws;
  attn_kernel<<<256, 512, 0, stream>>>(x, k, v, wq, wo, slab);
  reduce_kernel<<<224, 256, 0, stream>>>((const u32x4*)slab, (char*)d_out);
}
```

```cpp
#include <hip/hip_runtime.h>
#include <hip/hip_bf16.h>
#include <stdint.h>
#include <type_traits>

typedef __bf16 bf16x8 __attribute__((ext_vector_type(8)));
typedef __bf16 bf16x2 __attribute__((ext_vector_type(2)));
typedef short s16x4 __attribute__((ext_vector_type(4)));
typedef short s16x8 __attribute__((ext_vector_type(8)));
typedef float f32x2 __attribute__((ext_vector_type(2)));
typedef float f32x16 __attribute__((ext_vector_type(16)));
typedef unsigned u32x2 __attribute__((ext_vector_type(2)));
typedef unsigned u32x4 __attribute__((ext_vector_type(4)));
#define LAS __attribute__((address_space(3)))
#define DI __device__ __forceinline__

constexpr int NSTEP = 31;
constexpr int NSLOT = 4;
constexpr unsigned SLOT_BYTES = 32768;
constexpr unsigned SCR = NSLOT * SLOT_BYTES;

struct Sched { int slot[NSTEP]; int wait[NSTEP]; int issue_at[NSTEP]; };
constexpr int npieces(int t) { return t < 7 ? 10 : 8; }
constexpr Sched make_sched() {
  Sched s{};
  for (int t = 0; t < NSTEP; ++t) {
    s.slot[t] = t % NSLOT;
    int last = (t == 0) ? (NSLOT - 1) : (t + NSLOT - 2 < NSTEP - 1 ? t + NSLOT - 2 : NSTEP - 1);
    int w = 0;
    for (int u = t + 1; u <= last; ++u) w += npieces(u);
    s.wait[t] = w;
    s.issue_at[t] = (t >= 1 && t + NSLOT - 1 < NSTEP) ? t + NSLOT - 1 : -1;
  }
  return s;
}
constexpr Sched SCH = make_sched();

template <int B, int E, class F> DI void static_for(F&& f) {
  if constexpr (B < E) { f(std::integral_constant<int, B>{}); static_for<B + 1, E>(f); }
}

template <int N, int D, class L, class M> DI void pipe(L&& load, M&& mma) {
  bf16x8 fr[N];
  static_for<0, D>([&](auto ic) { fr[decltype(ic)::value] = load(ic); });
  __builtin_amdgcn_sched_barrier(0);
  static_for<0, N>([&](auto ic) {
    constexpr int i = decltype(ic)::value;
    if constexpr (i + D < N) fr[i + D] = load(std::integral_constant<int, i + D>{});
    mma(ic, fr[i]);
    __builtin_amdgcn_sched_barrier(0);
  });
}

DI void dma16(unsigned lds_addr, const char* sbase, unsigned voff) {
  asm volatile("s_mov_b32 m0, %0\n\ts_nop 0\n\tglobal_load_lds_dwordx4 %1, %2"
               :: "s"(lds_addr), "v"(voff), "s"(sbase) : "memory");
}
DI void dma16nt(unsigned lds_addr, const char* sbase, unsigned voff) {
  asm volatile("s_mov_b32 m0, %0\n\ts_nop 0\n\tglobal_load_lds_dwordx4 %1, %2 nt"
               :: "s"(lds_addr), "v"(voff), "s"(sbase) : "memory");
}
template <int N> DI void top_sync() {
  asm volatile("s_waitcnt vmcnt(%0) lgkmcnt(0)\n\ts_barrier" :: "n"(N) : "memory");
}
DI void lds_sync() { asm volatile("s_waitcnt lgkmcnt(0)\n\ts_barrier" ::: "memory"); }

DI unsigned pk2(float a, float b) {
  f32x2 v = {a, b};
  bf16x2 r = __builtin_convertvector(v, bf16x2);
  return __builtin_bit_cast(unsigned, r);
}
DI bf16x8 tr_frag(unsigned a0, unsigned a1) {
  s16x4 lo = __builtin_amdgcn_ds_read_tr16_b64_v4i16((LAS s16x4*)a0);
  s16x4 hi = __builtin_amdgcn_ds_read_tr16_b64_v4i16((LAS s16x4*)a1);
  s16x8 r = __builtin_shufflevector(lo, hi, 0, 1, 2, 3, 4, 5, 6, 7);
  return __builtin_bit_cast(bf16x8, r);
}
DI bf16x8 lds_b128(unsigned a) { return *(LAS const bf16x8*)a; }
DI float swap32_max(float v) {
  auto rr = __builtin_amdgcn_permlane32_swap(__float_as_uint(v), __float_as_uint(v), false, false);
  return fmaxf(__uint_as_float(rr[0]), __uint_as_float(rr[1]));
}
DI float swap32_add(float v) {
  auto rr = __builtin_amdgcn_permlane32_swap(__float_as_uint(v), __float_as_uint(v), false, false);
  return __uint_as_float(rr[0]) + __uint_as_float(rr[1]);
}

__global__ __launch_bounds__(512, 2) void attn_kernel(const char* __restrict__ x, const char* __restrict__ Kc,
                                                      const char* __restrict__ Vc, const char* __restrict__ wq,
                                                      const char* __restrict__ wo, char* __restrict__ slab) {
  __shared__ __attribute__((aligned(1024))) char smem[163840];
  const unsigned lds0 = (unsigned)(size_t)smem;
  const int tid = threadIdx.x, lane = tid & 63;
  const int wid8 = __builtin_amdgcn_readfirstlane(tid >> 6);
  const int wid = wid8 & 3;
  const int qb = wid & 1, role = wid >> 1;
  const int hi = lane >> 5, l31 = lane & 31;
  const int bid = blockIdx.x;
  const int xcd = bid & 7, idx = bid >> 3;
  const int qh = idx & 1, hl = (idx >> 1) & 1, b = idx >> 2, h = xcd * 2 + hl;

  if (wid8 >= 4) {
    const size_t kvo = (size_t)(b * 16 + h) * (512 * 512) + (size_t)((wid >> 1) * 256 + (wid & 1) * 16) * 512;
    const char* Kb = Kc + kvo;
    const char* Vb = Vc + kvo;
    const int rot = b;
    const char* Wqb = wq + (size_t)(wid * 16) * 8192 + h * 512;
    const char* Wob = wo + (size_t)(h * 256 + wid * 8) * 896;
    const char* Xb = x + (size_t)(b * 128 + qh * 64 + wid * 16) * 896;
    const unsigned ldsw = lds0 + wid * 8192;
    auto issue = [&](auto sc) {
      constexpr int S = decltype(sc)::value;
      constexpr unsigned sl = SCH.slot[S] * SLOT_BYTES;
      if constexpr (S < 7) {
        static_for<0, 2>([&](auto jc) {
          constexpr int j = decltype(jc)::value;
          unsigned voff = (lane >> 3) * 896 + (((lane & 7) ^ (lane >> 3) ^ j) << 4);
          dma16(lds0 + SCR + SCH.slot[S] * 8192 + (2 * wid + j) * 1024, Xb + (size_t)(8 * j) * 896 + 128 * S, voff);
        });
      }
      static_for<0, 8>([&](auto jc) {
        constexpr int j = decltype(jc)::value;
        if constexpr (S < 7) {
          unsigned voff = hi * 8192 + ((l31 ^ ((2 * (j & 1) + hi) << 2)) << 4);
          dma16(ldsw + sl + j * 1024, Wqb + (size_t)(64 * S + 2 * j) * 8192, voff);
        } else if constexpr (S < 15) {
          unsigned voff = hi * 512 + ((l31 ^ hi ^ (2 * j)) << 4);
          dma16nt(ldsw + sl + j * 1024, Kb + (size_t)(32 * ((S - 7 + rot) & 7) + 2 * j) * 512, voff);
        } else if constexpr (S < 23) {
          unsigned voff = hi * 512 + ((l31 ^ ((2 * (j & 1) + hi) << 2)) << 4);
          dma16nt(ldsw + sl + j * 1024, Vb + (size_t)(32 * ((S - 15 + rot) & 7) + 2 * j) * 512, voff);
        } else {
          unsigned g = lane ^ ((j & 3) << 2);
          g = (g >= 56) ? g - 8 : g;
          dma16(ldsw + sl + j * 1024, Wob + (size_t)(32 * (S - 23) + j) * 896, g << 4);
        }
      });
    };
    static_for<0, NSLOT>([&](auto ic) { issue(ic); });
    static_for<0, NSTEP>([&](auto sc) {
      constexpr int S = decltype(sc)::value;
      top_sync<SCH.wait[S]>();
      if constexpr (SCH.issue_at[S] >= 0) issue(std::integral_constant<int, SCH.issue_at[S]>{});
      if constexpr (S == 6 || S == 14) { asm volatile("s_barrier" ::: "memory"); }
      if constexpr (S == 22) { asm volatile("s_barrier" ::: "memory"); asm volatile("s_barrier" ::: "memory"); }
    });
    return;
  }

  auto top = [&](auto sc) { lds_sync(); };

  const int q4 = (lane & 15) >> 2, p = lane & 3, bb = (lane >> 4) & 1;
  unsigned trc[4];
#pragma unroll
  for (int blk = 0; blk < 4; ++blk) trc[blk] = ((16 * role + 4 * (blk ^ q4) + 2 * bb + (p >> 1)) << 4) + 8 * (p & 1);

  f32x16 acc[4];
#pragma unroll
  for (int i = 0; i < 4; ++i) acc[i] = f32x16{};
  const unsigned xr = lds0 + SCR + (4 * qb + (l31 >> 3)) * 1024 + (l31 & 7) * 128;
  const unsigned xsw = (l31 & 7) ^ ((l31 >> 3) & 1) ^ hi;
  static_for<0, 7>([&](auto tc) {
    constexpr int t = decltype(tc)::value;
    top(tc);
    constexpr unsigned sl = SCH.slot[t] * SLOT_BYTES;
    const unsigned rb = lds0 + sl + (8 * hi + q4) * 512;
    bf16x8 xf[4];
#pragma unroll
    for (int ks = 0; ks < 4; ++ks) xf[ks] = lds_b128(xr + SCH.slot[t] * 8192 + (((2 * ks) ^ xsw) << 4));
    pipe<16, 4>(
        [&](auto ic) { constexpr int i = decltype(ic)::value; constexpr int ks = i >> 2, blk = i & 3;
                       return tr_frag(rb + trc[blk] + (16 * ks) * 512, rb + trc[blk] + (16 * ks + 4) * 512); },
        [&](auto ic, bf16x8 a) { constexpr int i = decltype(ic)::value; constexpr int ks = i >> 2, blk = i & 3;
                       acc[blk] = __builtin_amdgcn_mfma_f32_32x32x16_bf16(a, xf[ks], acc[blk], 0, 0, 0); });
  });
  lds_sync();
  const unsigned qrow = lds0 + SCR + (32 * qb + l31) * 512;
#pragma unroll
  for (int blk = 0; blk < 4; ++blk)
#pragma unroll
    for (int g = 0; g < 4; ++g) {
      u32x2 w = {pk2(acc[blk][4 * g], acc[blk][4 * g + 1]), pk2(acc[blk][4 * g + 2], acc[blk][4 * g + 3])};
      *(LAS u32x2*)(qrow + (((16 * role + 4 * blk + g) ^ (l31 & 15)) << 4) + 8 * hi) = w;
    }

  f32x16 S[8];
  bf16x8 qf[16];
  float mx = -3.0e38f;
  static_for<0, 8>([&](auto tc) {
    constexpr int t = decltype(tc)::value;
    top(std::integral_constant<int, 7 + t>{});
    if constexpr (t == 0) {
#pragma unroll
      for (int s = 0; s < 16; ++s) qf[s] = lds_b128(qrow + (((2 * s + hi) ^ (l31 & 15)) << 4));
    }
    constexpr unsigned sl = SCH.slot[7 + t] * SLOT_BYTES;
    const unsigned kr = lds0 + sl + (32 * role + l31) * 512;
    f32x16 c = f32x16{};
    pipe<16, 4>(
        [&](auto ic) { constexpr int s = decltype(ic)::value; return lds_b128(kr + ((((2 * (s & 7)) + hi) ^ (l31 & 15)) << 4) + (s >> 3) * 256); },
        [&](auto ic, bf16x8 a) { constexpr int s = decltype(ic)::value;
                       c = __builtin_amdgcn_mfma_f32_32x32x16_bf16(a, qf[s], c, 0, 0, 0); });
    S[t] = c;
#pragma unroll
    for (int r = 0; r < 16; ++r) mx = fmaxf(mx, c[r]);
  });
  mx = swap32_max(mx);
  const unsigned xbuf = lds0 + SCR + 16384;
  *(LAS float*)(xbuf + wid * 256 + lane * 4) = mx;
  lds_sync();
  mx = fmaxf(mx, *(LAS const float*)(xbuf + (wid ^ 2) * 256 + lane * 4));

  constexpr float C = 0.0625f * 1.44269504088896340736f;
  const float mC = -mx * C;
  float lsum = 0.f;
  const unsigned pbuf = lds0 + SCR;
  bf16x8 pown[2];
  auto make_p = [&](const f32x16& s, int par) {
    float pe[16];
#pragma unroll
    for (int r = 0; r < 16; ++r) { pe[r] = __builtin_amdgcn_exp2f(fmaf(s[r], C, mC)); lsum += pe[r]; }
#pragma unroll
    for (int sp = 0; sp < 2; ++sp) {
      u32x4 w = {pk2(pe[8 * sp], pe[8 * sp + 1]), pk2(pe[8 * sp + 2], pe[8 * sp + 3]),
                 pk2(pe[8 * sp + 4], pe[8 * sp + 5]), pk2(pe[8 * sp + 6], pe[8 * sp + 7])};
      pown[sp] = __builtin_bit_cast(bf16x8, w);
      *(LAS u32x4*)(pbuf + par * 8192 + wid * 2048 + sp * 1024 + lane * 16) = w;
    }
  };
  make_p(S[0], 0);

  f32x16 o[4];
#pragma unroll
  for (int i = 0; i < 4; ++i) o[i] = f32x16{};
  static_for<0, 8>([&](auto tc) {
    constexpr int t = decltype(tc)::value;
    top(std::integral_constant<int, 15 + t>{});
    constexpr unsigned sl = SCH.slot[15 + t] * SLOT_BYTES;
    bf16x8 pf[2][2];
    pf[0][0] = pown[0]; pf[0][1] = pown[1];
    pf[1][0] = lds_b128(pbuf + (t & 1) * 8192 + (wid ^ 2) * 2048 + lane * 16);
    pf[1][1] = lds_b128(pbuf + (t & 1) * 8192 + (wid ^ 2) * 2048 + 1024 + lane * 16);
    const unsigned vr0 = lds0 + sl + (32 * role + 4 * hi + q4) * 512;
    const unsigned vr1 = lds0 + sl + (32 * (role ^ 1) + 4 * hi + q4) * 512;
    float pe[16];
    unsigned pk[8];
    pipe<16, 4>(
        [&](auto ic) { constexpr int i = decltype(ic)::value; constexpr int u = i >> 3, sp = (i >> 2) & 1, blk = i & 3;
                       const unsigned vr = u ? vr1 : vr0;
                       return tr_frag(vr + trc[blk] + (16 * sp) * 512, vr + trc[blk] + (16 * sp + 8) * 512); },
        [&](auto ic, bf16x8 a) { constexpr int i = decltype(ic)::value; constexpr int u = i >> 3, sp = (i >> 2) & 1, blk = i & 3;
                       o[blk] = __builtin_amdgcn_mfma_f32_32x32x16_bf16(a, pf[u][sp], o[blk], 0, 0, 0);
                       if constexpr (t < 7) {
                         pe[i] = __builtin_amdgcn_exp2f(fmaf(S[t + 1][i], C, mC)); lsum += pe[i];
                         if constexpr (i & 1) pk[i >> 1] = pk2(pe[i - 1], pe[i]);
                         if constexpr ((i & 7) == 7) {
                           constexpr int s4 = (i >> 3) * 4;
                           u32x4 w = {pk[s4], pk[s4 + 1], pk[s4 + 2], pk[s4 + 3]};
                           pown[i >> 3] = __builtin_bit_cast(bf16x8, w);
                           *(LAS u32x4*)(pbuf + ((t + 1) & 1) * 8192 + wid * 2048 + (i >> 3) * 1024 + lane * 16) = w;
                         }
                       } });
    if constexpr (t == 7) {
      float l2 = swap32_add(lsum);
      *(LAS float*)(xbuf + wid * 256 + lane * 4) = l2;
      lsum = l2;
    }
  });

  bf16x8 ofo[4][2], ofp[4][2];
  const unsigned obuf = lds0 + SCR + 0;
  f32x16 acc2[7];
#pragma unroll
  for (int i = 0; i < 7; ++i) acc2[i] = f32x16{};
  lds_sync();
  {
    float lt = lsum + *(LAS const float*)(xbuf + (wid ^ 2) * 256 + lane * 4);
    float inv = 1.0f / lt;
#pragma unroll
    for (int blk = 0; blk < 4; ++blk)
#pragma unroll
      for (int sp = 0; sp < 2; ++sp) {
        u32x4 w = {pk2(o[blk][8 * sp] * inv, o[blk][8 * sp + 1] * inv), pk2(o[blk][8 * sp + 2] * inv, o[blk][8 * sp + 3] * inv),
                   pk2(o[blk][8 * sp + 4] * inv, o[blk][8 * sp + 5] * inv), pk2(o[blk][8 * sp + 6] * inv, o[blk][8 * sp + 7] * inv)};
        ofo[blk][sp] = __builtin_bit_cast(bf16x8, w);
      }
  }
  lds_sync();
#pragma unroll
  for (int blk = 0; blk < 4; ++blk)
#pragma unroll
    for (int sp = 0; sp < 2; ++sp)
      *(LAS u32x4*)(obuf + wid * 8192 + (blk * 2 + sp) * 1024 + lane * 16) = __builtin_bit_cast(u32x4, ofo[blk][sp]);

  unsigned trw[7];
#pragma unroll
  for (int nb = 0; nb < 7; ++nb) trw[nb] = ((4 * ((7 * role + nb) ^ q4) + 2 * bb + (p >> 1)) << 4) + 8 * (p & 1);
  static_for<0, 8>([&](auto tc) {
    constexpr int t = decltype(tc)::value;
    top(std::integral_constant<int, 23 + t>{});
    if constexpr (t == 0) {
#pragma unroll
      for (int blk = 0; blk < 4; ++blk)
#pragma unroll
        for (int sp = 0; sp < 2; ++sp)
          ofp[blk][sp] = lds_b128(obuf + (wid ^ 2) * 8192 + (blk * 2 + sp) * 1024 + lane * 16);
    }
    constexpr unsigned sl = SCH.slot[23 + t] * SLOT_BYTES;
    const unsigned wr = lds0 + sl + (4 * hi + q4) * 1024;
    const bf16x8 bf0 = ((t >> 2) == role) ? ofo[t & 3][0] : ofp[t & 3][0];
    const bf16x8 bf1 = ((t >> 2) == role) ? ofo[t & 3][1] : ofp[t & 3][1];
    pipe<14, 4>(
        [&](auto ic) { constexpr int i = decltype(ic)::value; constexpr int sp = i / 7, nb = i % 7;
                       return tr_frag(wr + trw[nb] + (16 * sp) * 1024, wr + trw[nb] + (16 * sp + 8) * 1024); },
        [&](auto ic, bf16x8 a) { constexpr int i = decltype(ic)::value; constexpr int sp = i / 7, nb = i % 7;
                       acc2[nb] = __builtin_amdgcn_mfma_f32_32x32x16_bf16(a, sp ? bf1 : bf0, acc2[nb], 0, 0, 0); });
  });

  char* sp_ = slab + ((size_t)(((h * 8 + b) * 2 + qh) * 4 + wid) * 14) * 1024 + lane * 16;
#pragma unroll
  for (int nb = 0; nb < 7; ++nb)
#pragma unroll
    for (int g2 = 0; g2 < 2; ++g2) {
      u32x4 w = {pk2(acc2[nb][8 * g2], acc2[nb][8 * g2 + 1]), pk2(acc2[nb][8 * g2 + 2], acc2[nb][8 * g2 + 3]),
                 pk2(acc2[nb][8 * g2 + 4], acc2[nb][8 * g2 + 5]), pk2(acc2[nb][8 * g2 + 6], acc2[nb][8 * g2 + 7])};
      *(u32x4*)(sp_ + (nb * 2 + g2) * 1024) = w;
    }
}

__global__ __launch_bounds__(256) void reduce_kernel(const u32x4* __restrict__ slab, char* __restrict__ out) {
  const int i = blockIdx.x * 256 + threadIdx.x;
  float s[8];
#pragma unroll
  for (int k = 0; k < 8; ++k) s[k] = 0.f;
  u32x4 v[16];
#pragma unroll
  for (int h = 0; h < 16; ++h) v[h] = slab[(size_t)h * 57344 + i];
#pragma unroll
  for (int h = 0; h < 16; ++h)
#pragma unroll
    for (int k = 0; k < 4; ++k) {
      s[2 * k] += __uint_as_float(v[h][k] << 16);
      s[2 * k + 1] += __uint_as_float(v[h][k] & 0xffff0000u);
    }
  const int lane = i & 63, g2 = (i >> 6) & 1;
  int rest = i >> 7;
  const int nb = rest % 7; rest /= 7;
  const int wid = rest & 3, qh = (rest >> 2) & 1, b = rest >> 3;
  const int row = b * 128 + qh * 64 + (wid & 1) * 32 + (lane & 31);
  const int n0 = 32 * (7 * (wid >> 1) + nb) + 16 * g2 + 4 * (lane >> 5);
  char* o = out + ((size_t)row * 448 + n0) * 2;
  u32x2 w0 = {pk2(s[0], s[1]), pk2(s[2], s[3])};
  u32x2 w1 = {pk2(s[4], s[5]), pk2(s[6], s[7])};
  *(u32x2*)o = w0;
  *(u32x2*)(o + 16) = w1;
}

extern "C" void kernel_launch(void* const* d_in, const int* in_sizes, int n_in, void* d_out, int out_size,
                              void* d_ws, size_t ws_size, hipStream_t stream) {
  const char* x = (const char*)d_in[0];
  const char* k = (const char*)d_in[1];
  const char* v = (const char*)d_in[2];
  const char* wq = (const char*)d_in[3];
  const char* wo = (const char*)d_in[4];
  char* slab = (char*)d_ws;
  attn_kernel<<<256, 512, 0, stream>>>(x, k, v, wq, wo, slab);
  reduce_kernel<<<224, 256, 0, stream>>>((const u32x4*)slab, (char*)d_out);
}
```

```cpp
#define KPF 0
#include <hip/hip_runtime.h>
#include <hip/hip_bf16.h>
#include <stdint.h>
#include <type_traits>

typedef __bf16 bf16x8 __attribute__((ext_vector_type(8)));
typedef __bf16 bf16x2 __attribute__((ext_vector_type(2)));
typedef short s16x4 __attribute__((ext_vector_type(4)));
typedef short s16x8 __attribute__((ext_vector_type(8)));
typedef float f32x2 __attribute__((ext_vector_type(2)));
typedef float f32x16 __attribute__((ext_vector_type(16)));
typedef unsigned u32x2 __attribute__((ext_vector_type(2)));
typedef unsigned u32x4 __attribute__((ext_vector_type(4)));
#define LAS __attribute__((address_space(3)))
#define DI __device__ __forceinline__

constexpr int NSTEP = 31;
constexpr int NSLOT = 4;
constexpr unsigned SLOT_BYTES = 32768;
constexpr unsigned SCR = NSLOT * SLOT_BYTES;

struct Sched { int slot[NSTEP]; int wait[NSTEP]; int issue_at[NSTEP]; };
constexpr int npieces(int t) { return t < 7 ? 10 : 8; }
constexpr Sched make_sched() {
  Sched s{};
  for (int t = 0; t < NSTEP; ++t) {
    s.slot[t] = t % NSLOT;
    int last = (t == 0) ? 1 : (t + NSLOT - 2 < NSTEP - 1 ? t + NSLOT - 2 : NSTEP - 1);
    int w = 0;
    for (int u = t + 1; u <= last; ++u) w += npieces(u);
    s.wait[t] = w;
    s.issue_at[t] = (t >= 1 && t + NSLOT - 1 < NSTEP) ? t + NSLOT - 1 : -1;
  }
  return s;
}
constexpr Sched SCH = make_sched();

template <int B, int E, class F> DI void static_for(F&& f) {
  if constexpr (B < E) { f(std::integral_constant<int, B>{}); static_for<B + 1, E>(f); }
}

template <int N, int D, class L, class M> DI void pipe(L&& load, M&& mma) {
  bf16x8 fr[N];
  static_for<0, D>([&](auto ic) { fr[decltype(ic)::value] = load(ic); });
  __builtin_amdgcn_sched_barrier(0);
  static_for<0, N>([&](auto ic) {
    constexpr int i = decltype(ic)::value;
    if constexpr (i + D < N) fr[i + D] = load(std::integral_constant<int, i + D>{});
    mma(ic, fr[i]);
    __builtin_amdgcn_sched_barrier(0);
  });
}

DI void dma16(unsigned lds_addr, const char* sbase, unsigned voff) {
  asm volatile("s_mov_b32 m0, %0\n\ts_nop 0\n\tglobal_load_lds_dwordx4 %1, %2"
               :: "s"(lds_addr), "v"(voff), "s"(sbase) : "memory");
}
DI void dma16nt(unsigned lds_addr, const char* sbase, unsigned voff) {
  asm volatile("s_mov_b32 m0, %0\n\ts_nop 0\n\tglobal_load_lds_dwordx4 %1, %2 nt"
               :: "s"(lds_addr), "v"(voff), "s"(sbase) : "memory");
}
template <int N> DI void top_sync() {
  asm volatile("s_waitcnt vmcnt(%0) lgkmcnt(0)\n\ts_barrier" :: "n"(N) : "memory");
}
DI void lds_sync() { asm volatile("s_waitcnt lgkmcnt(0)\n\ts_barrier" ::: "memory"); }

DI unsigned pk2(float a, float b) {
  f32x2 v = {a, b};
  bf16x2 r = __builtin_convertvector(v, bf16x2);
  return __builtin_bit_cast(unsigned, r);
}
DI bf16x8 tr_frag(unsigned a0, unsigned a1) {
  s16x4 lo = __builtin_amdgcn_ds_read_tr16_b64_v4i16((LAS s16x4*)a0);
  s16x4 hi = __builtin_amdgcn_ds_read_tr16_b64_v4i16((LAS s16x4*)a1);
  s16x8 r = __builtin_shufflevector(lo, hi, 0, 1, 2, 3, 4, 5, 6, 7);
  return __builtin_bit_cast(bf16x8, r);
}
DI bf16x8 lds_b128(unsigned a) { return *(LAS const bf16x8*)a; }
DI float swap32_max(float v) {
  auto rr = __builtin_amdgcn_permlane32_swap(__float_as_uint(v), __float_as_uint(v), false, false);
  return fmaxf(__uint_as_float(rr[0]), __uint_as_float(rr[1]));
}
DI float swap32_add(float v) {
  auto rr = __builtin_amdgcn_permlane32_swap(__float_as_uint(v), __float_as_uint(v), false, false);
  return __uint_as_float(rr[0]) + __uint_as_float(rr[1]);
}

__global__ __launch_bounds__(512, 2) void attn_kernel(const char* __restrict__ x, const char* __restrict__ Kc,
                                                      const char* __restrict__ Vc, const char* __restrict__ wq,
                                                      const char* __restrict__ wo, char* __restrict__ slab) {
  __shared__ __attribute__((aligned(1024))) char smem[163840];
  const unsigned lds0 = (unsigned)(size_t)smem;
  const int tid = threadIdx.x, lane = tid & 63;
  const int wid8 = __builtin_amdgcn_readfirstlane(tid >> 6);
  const int wid = wid8 & 3;
  const int qb = wid & 1, role = wid >> 1;
  const int hi = lane >> 5, l31 = lane & 31;
  const int bid = blockIdx.x;
  const int xcd = bid & 7, idx = bid >> 3;
  const int qh = idx & 1, hl = (idx >> 1) & 1, b = idx >> 2, h = xcd * 2 + hl;

  if (wid8 >= 4) {
    const size_t kvo = (size_t)(b * 16 + h) * (512 * 512) + (size_t)((wid >> 1) * 256 + (wid & 1) * 16) * 512;
    const char* Kb = Kc + kvo;
    const char* Vb = Vc + kvo;
    const int rot = b;
    const char* Wqb = wq + (size_t)(wid * 16) * 8192 + h * 512;
    const char* Wob = wo + (size_t)(h * 256 + wid * 8) * 896;
    const char* Xb = x + (size_t)(b * 128 + qh * 64 + wid * 16) * 896;
    const unsigned ldsw = lds0 + wid * 8192;
    auto issue = [&](auto sc) {
      constexpr int S = decltype(sc)::value;
      constexpr unsigned sl = SCH.slot[S] * SLOT_BYTES;
      if constexpr (S < 7) {
        static_for<0, 2>([&](auto jc) {
          constexpr int j = decltype(jc)::value;
          unsigned voff = (lane >> 3) * 896 + (((lane & 7) ^ (lane >> 3) ^ j) << 4);
          dma16(lds0 + SCR + SCH.slot[S] * 8192 + (2 * wid + j) * 1024, Xb + (size_t)(8 * j) * 896 + 128 * S, voff);
        });
      }
      static_for<0, 8>([&](auto jc) {
        constexpr int j = decltype(jc)::value;
        if constexpr (S < 7) {
          unsigned voff = hi * 8192 + ((l31 ^ ((2 * (j & 1) + hi) << 2)) << 4);
          dma16(ldsw + sl + j * 1024, Wqb + (size_t)(64 * S + 2 * j) * 8192, voff);
        } else if constexpr (S < 15) {
          unsigned voff = hi * 512 + ((l31 ^ hi ^ (2 * j)) << 4);
          dma16nt(ldsw + sl + j * 1024, Kb + (size_t)(32 * ((S - 7 + rot) & 7) + 2 * j) * 512, voff);
        } else if constexpr (S < 23) {
          unsigned voff = hi * 512 + ((l31 ^ ((2 * (j & 1) + hi) << 2)) << 4);
          dma16nt(ldsw + sl + j * 1024, Vb + (size_t)(32 * ((S - 15 + rot) & 7) + 2 * j) * 512, voff);
        } else {
          unsigned g = lane ^ ((j & 3) << 2);
          g = (g >= 56) ? g - 8 : g;
          dma16(ldsw + sl + j * 1024, Wob + (size_t)(32 * (S - 23) + j) * 896, g << 4);
        }
      });
    };
    static_for<0, 2>([&](auto ic) { issue(ic); });
    static_for<0, NSTEP>([&](auto sc) {
      constexpr int S = decltype(sc)::value;
      top_sync<SCH.wait[S]>();
      if constexpr (S == 0) { issue(std::integral_constant<int, 2>{}); issue(std::integral_constant<int, 3>{}); }
      if constexpr (SCH.issue_at[S] >= 0) issue(std::integral_constant<int, SCH.issue_at[S]>{});
      if constexpr (S == 6 || S == 14) { asm volatile("s_barrier" ::: "memory"); }
      if constexpr (S == 22) { asm volatile("s_barrier" ::: "memory"); asm volatile("s_barrier" ::: "memory"); }
    });
    return;
  }

  {
    unsigned t0, t1, t2, t3;
    const char* pb; unsigned o0, o1;
    if (wid == 0) {
      pb = wq + (size_t)(idx * 14) * 8192 + xcd * 1024;
      o0 = (lane >> 3) * 8192 + (lane & 7) * 128;
      o1 = ((lane >> 3) + 8 < 14 ? (lane >> 3) + 8 : 13) * 8192 + (lane & 7) * 128;
    } else if (wid == 1) {
      pb = wo + (size_t)(xcd * 512 + idx * 16) * 896;
      o0 = lane * 128;
      o1 = (lane + 64 < 112 ? lane + 64 : 111) * 128;
    } else {
      pb = x + (size_t)(b * 128 + qh * 64) * 896 + hl * (224 * 128) + (wid - 2) * (128 * 128);
      o0 = lane * 128;
      o1 = ((wid == 3 && lane >= 32) ? 31 + 64 : lane + 64) * 128;
    }
    asm volatile("global_load_dword %0, %1, %2" : "=v"(t0) : "v"(o0), "s"(pb) : "memory");
    asm volatile("global_load_dword %0, %1, %2" : "=v"(t1) : "v"(o1), "s"(pb) : "memory");
    asm volatile("s_waitcnt vmcnt(0)" :: "v"(t0), "v"(t1) : "memory");
  }
  auto top = [&](auto sc) { lds_sync(); };

  const int q4 = (lane & 15) >> 2, p = lane & 3, bb = (lane >> 4) & 1;
  unsigned trc[4];
#pragma unroll
  for (int blk = 0; blk < 4; ++blk) trc[blk] = ((16 * role + 4 * (blk ^ q4) + 2 * bb + (p >> 1)) << 4) + 8 * (p & 1);

  f32x16 acc[4];
#pragma unroll
  for (int i = 0; i < 4; ++i) acc[i] = f32x16{};
  const unsigned xr = lds0 + SCR + (4 * qb + (l31 >> 3)) * 1024 + (l31 & 7) * 128;
  const unsigned xsw = (l31 & 7) ^ ((l31 >> 3) & 1) ^ hi;
  static_for<0, 7>([&](auto tc) {
    constexpr int t = decltype(tc)::value;
    top(tc);
    constexpr unsigned sl = SCH.slot[t] * SLOT_BYTES;
    const unsigned rb = lds0 + sl + (8 * hi + q4) * 512;
    bf16x8 xf[4];
#pragma unroll
    for (int ks = 0; ks < 4; ++ks) xf[ks] = lds_b128(xr + SCH.slot[t] * 8192 + (((2 * ks) ^ xsw) << 4));
    pipe<16, 4>(
        [&](auto ic) { constexpr int i = decltype(ic)::value; constexpr int ks = i >> 2, blk = i & 3;
                       return tr_frag(rb + trc[blk] + (16 * ks) * 512, rb + trc[blk] + (16 * ks + 4) * 512); },
        [&](auto ic, bf16x8 a) { constexpr int i = decltype(ic)::value; constexpr int ks = i >> 2, blk = i & 3;
                       acc[blk] = __builtin_amdgcn_mfma_f32_32x32x16_bf16(a, xf[ks], acc[blk], 0, 0, 0); });
  });
  lds_sync();
  const unsigned qrow = lds0 + SCR + (32 * qb + l31) * 512;
#pragma unroll
  for (int blk = 0; blk < 4; ++blk)
#pragma unroll
    for (int g = 0; g < 4; ++g) {
      u32x2 w = {pk2(acc[blk][4 * g], acc[blk][4 * g + 1]), pk2(acc[blk][4 * g + 2], acc[blk][4 * g + 3])};
      *(LAS u32x2*)(qrow + (((16 * role + 4 * blk + g) ^ (l31 & 15)) << 4) + 8 * hi) = w;
    }

  f32x16 S[8];
  bf16x8 qf[16];
  float mx = -3.0e38f;
  static_for<0, 8>([&](auto tc) {
    constexpr int t = decltype(tc)::value;
    top(std::integral_constant<int, 7 + t>{});
    if constexpr (t == 0) {
#pragma unroll
      for (int s = 0; s < 16; ++s) qf[s] = lds_b128(qrow + (((2 * s + hi) ^ (l31 & 15)) << 4));
    }
    constexpr unsigned sl = SCH.slot[7 + t] * SLOT_BYTES;
    const unsigned kr = lds0 + sl + (32 * role + l31) * 512;
    f32x16 c = f32x16{};
    pipe<16, 4>(
        [&](auto ic) { constexpr int s = decltype(ic)::value; return lds_b128(kr + ((((2 * (s & 7)) + hi) ^ (l31 & 15)) << 4) + (s >> 3) * 256); },
        [&](auto ic, bf16x8 a) { constexpr int s = decltype(ic)::value;
                       c = __builtin_amdgcn_mfma_f32_32x32x16_bf16(a, qf[s], c, 0, 0, 0); });
    S[t] = c;
#pragma unroll
    for (int r = 0; r < 16; ++r) mx = fmaxf(mx, c[r]);
  });
  mx = swap32_max(mx);
  const unsigned xbuf = lds0 + SCR + 16384;
  *(LAS float*)(xbuf + wid * 256 + lane * 4) = mx;
  lds_sync();
  mx = fmaxf(mx, *(LAS const float*)(xbuf + (wid ^ 2) * 256 + lane * 4));

  constexpr float C = 0.0625f * 1.44269504088896340736f;
  const float mC = -mx * C;
  float lsum = 0.f;
  const unsigned pbuf = lds0 + SCR;
  bf16x8 pown[2];
  auto make_p = [&](const f32x16& s, int par) {
    float pe[16];
#pragma unroll
    for (int r = 0; r < 16; ++r) { pe[r] = __builtin_amdgcn_exp2f(fmaf(s[r], C, mC)); lsum += pe[r]; }
#pragma unroll
    for (int sp = 0; sp < 2; ++sp) {
      u32x4 w = {pk2(pe[8 * sp], pe[8 * sp + 1]), pk2(pe[8 * sp + 2], pe[8 * sp + 3]),
                 pk2(pe[8 * sp + 4], pe[8 * sp + 5]), pk2(pe[8 * sp + 6], pe[8 * sp + 7])};
      pown[sp] = __builtin_bit_cast(bf16x8, w);
      *(LAS u32x4*)(pbuf + par * 8192 + wid * 2048 + sp * 1024 + lane * 16) = w;
    }
  };
  make_p(S[0], 0);

  f32x16 o[4];
#pragma unroll
  for (int i = 0; i < 4; ++i) o[i] = f32x16{};
  static_for<0, 8>([&](auto tc) {
    constexpr int t = decltype(tc)::value;
    top(std::integral_constant<int, 15 + t>{});
    constexpr unsigned sl = SCH.slot[15 + t] * SLOT_BYTES;
    bf16x8 pf[2][2];
    pf[0][0] = pown[0]; pf[0][1] = pown[1];
    pf[1][0] = lds_b128(pbuf + (t & 1) * 8192 + (wid ^ 2) * 2048 + lane * 16);
    pf[1][1] = lds_b128(pbuf + (t & 1) * 8192 + (wid ^ 2) * 2048 + 1024 + lane * 16);
    const unsigned vr0 = lds0 + sl + (32 * role + 4 * hi + q4) * 512;
    const unsigned vr1 = lds0 + sl + (32 * (role ^ 1) + 4 * hi + q4) * 512;
    float pe[16];
    unsigned pk[8];
    pipe<16, 4>(
        [&](auto ic) { constexpr int i = decltype(ic)::value; constexpr int u = i >> 3, sp = (i >> 2) & 1, blk = i & 3;
                       const unsigned vr = u ? vr1 : vr0;
                       return tr_frag(vr + trc[blk] + (16 * sp) * 512, vr + trc[blk] + (16 * sp + 8) * 512); },
        [&](auto ic, bf16x8 a) { constexpr int i = decltype(ic)::value; constexpr int u = i >> 3, sp = (i >> 2) & 1, blk = i & 3;
                       o[blk] = __builtin_amdgcn_mfma_f32_32x32x16_bf16(a, pf[u][sp], o[blk], 0, 0, 0);
                       if constexpr (t < 7) {
                         pe[i] = __builtin_amdgcn_exp2f(fmaf(S[t + 1][i], C, mC)); lsum += pe[i];
                         if constexpr (i & 1) pk[i >> 1] = pk2(pe[i - 1], pe[i]);
                         if constexpr ((i & 7) == 7) {
                           constexpr int s4 = (i >> 3) * 4;
                           u32x4 w = {pk[s4], pk[s4 + 1], pk[s4 + 2], pk[s4 + 3]};
                           pown[i >> 3] = __builtin_bit_cast(bf16x8, w);
                           *(LAS u32x4*)(pbuf + ((t + 1) & 1) * 8192 + wid * 2048 + (i >> 3) * 1024 + lane * 16) = w;
                         }
                       } });
    if constexpr (t == 7) {
      float l2 = swap32_add(lsum);
      *(LAS float*)(xbuf + wid * 256 + lane * 4) = l2;
      lsum = l2;
    }
  });

  bf16x8 ofo[4][2], ofp[4][2];
  const unsigned obuf = lds0 + SCR + 0;
  f32x16 acc2[7];
#pragma unroll
  for (int i = 0; i < 7; ++i) acc2[i] = f32x16{};
  lds_sync();
  {
    float lt = lsum + *(LAS const float*)(xbuf + (wid ^ 2) * 256 + lane * 4);
    float inv = 1.0f / lt;
#pragma unroll
    for (int blk = 0; blk < 4; ++blk)
#pragma unroll
      for (int sp = 0; sp < 2; ++sp) {
        u32x4 w = {pk2(o[blk][8 * sp] * inv, o[blk][8 * sp + 1] * inv), pk2(o[blk][8 * sp + 2] * inv, o[blk][8 * sp + 3] * inv),
                   pk2(o[blk][8 * sp + 4] * inv, o[blk][8 * sp + 5] * inv), pk2(o[blk][8 * sp + 6] * inv, o[blk][8 * sp + 7] * inv)};
        ofo[blk][sp] = __builtin_bit_cast(bf16x8, w);
      }
  }
  lds_sync();
#pragma unroll
  for (int blk = 0; blk < 4; ++blk)
#pragma unroll
    for (int sp = 0; sp < 2; ++sp)
      *(LAS u32x4*)(obuf + wid * 8192 + (blk * 2 + sp) * 1024 + lane * 16) = __builtin_bit_cast(u32x4, ofo[blk][sp]);

  unsigned trw[7];
#pragma unroll
  for (int nb = 0; nb < 7; ++nb) trw[nb] = ((4 * ((7 * role + nb) ^ q4) + 2 * bb + (p >> 1)) << 4) + 8 * (p & 1);
  static_for<0, 8>([&](auto tc) {
    constexpr int t = decltype(tc)::value;
    top(std::integral_constant<int, 23 + t>{});
    if constexpr (t == 0) {
#pragma unroll
      for (int blk = 0; blk < 4; ++blk)
#pragma unroll
        for (int sp = 0; sp < 2; ++sp)
          ofp[blk][sp] = lds_b128(obuf + (wid ^ 2) * 8192 + (blk * 2 + sp) * 1024 + lane * 16);
    }
    constexpr unsigned sl = SCH.slot[23 + t] * SLOT_BYTES;
    const unsigned wr = lds0 + sl + (4 * hi + q4) * 1024;
    const bf16x8 bf0 = ((t >> 2) == role) ? ofo[t & 3][0] : ofp[t & 3][0];
    const bf16x8 bf1 = ((t >> 2) == role) ? ofo[t & 3][1] : ofp[t & 3][1];
    pipe<14, 4>(
        [&](auto ic) { constexpr int i = decltype(ic)::value; constexpr int sp = i / 7, nb = i % 7;
                       return tr_frag(wr + trw[nb] + (16 * sp) * 1024, wr + trw[nb] + (16 * sp + 8) * 1024); },
        [&](auto ic, bf16x8 a) { constexpr int i = decltype(ic)::value; constexpr int sp = i / 7, nb = i % 7;
                       acc2[nb] = __builtin_amdgcn_mfma_f32_32x32x16_bf16(a, sp ? bf1 : bf0, acc2[nb], 0, 0, 0); });
  });

  char* sp_ = slab + ((size_t)(((h * 8 + b) * 2 + qh) * 4 + wid) * 14) * 1024 + lane * 16;
#pragma unroll
  for (int nb = 0; nb < 7; ++nb)
#pragma unroll
    for (int g2 = 0; g2 < 2; ++g2) {
      u32x4 w = {pk2(acc2[nb][8 * g2], acc2[nb][8 * g2 + 1]), pk2(acc2[nb][8 * g2 + 2], acc2[nb][8 * g2 + 3]),
                 pk2(acc2[nb][8 * g2 + 4], acc2[nb][8 * g2 + 5]), pk2(acc2[nb][8 * g2 + 6], acc2[nb][8 * g2 + 7])};
      *(u32x4*)(sp_ + (nb * 2 + g2) * 1024) = w;
    }
}

__global__ __launch_bounds__(256) void reduce_kernel(const u32x4* __restrict__ slab, char* __restrict__ out) {
  const int i = blockIdx.x * 256 + threadIdx.x;
  float s[8];
#pragma unroll
  for (int k = 0; k < 8; ++k) s[k] = 0.f;
  u32x4 v[16];
#pragma unroll
  for (int h = 0; h < 16; ++h) v[h] = slab[(size_t)h * 57344 + i];
#pragma unroll
  for (int h = 0; h < 16; ++h)
#pragma unroll
    for (int k = 0; k < 4; ++k) {
      s[2 * k] += __uint_as_float(v[h][k] << 16);
      s[2 * k + 1] += __uint_as_float(v[h][k] & 0xffff0000u);
    }
  const int lane = i & 63, g2 = (i >> 6) & 1;
  int rest = i >> 7;
  const int nb = rest % 7; rest /= 7;
  const int wid = rest & 3, qh = (rest >> 2) & 1, b = rest >> 3;
  const int row = b * 128 + qh * 64 + (wid & 1) * 32 + (lane & 31);
  const int n0 = 32 * (7 * (wid >> 1) + nb) + 16 * g2 + 4 * (lane >> 5);
  char* o = out + ((size_t)row * 448 + n0) * 2;
  u32x2 w0 = {pk2(s[0], s[1]), pk2(s[2], s[3])};
  u32x2 w1 = {pk2(s[4], s[5]), pk2(s[6], s[7])};
  *(u32x2*)o = w0;
  *(u32x2*)(o + 16) = w1;
}

extern "C" void kernel_launch(void* const* d_in, const int* in_sizes, int n_in, void* d_out, int out_size,
                              void* d_ws, size_t ws_size, hipStream_t stream) {
  const char* x = (const char*)d_in[0];
  const char* k = (const char*)d_in[1];
  const char* v = (const char*)d_in[2];
  const char* wq = (const char*)d_in[3];
  const char* wo = (const char*)d_in[4];
  char* slab = (char*)d_ws;
  attn_kernel<<<256, 512, 0, stream>>>(x, k, v, wq, wo, slab);
  reduce_kernel<<<224, 256, 0, stream>>>((const u32x4*)slab, (char*)d_out);
}
```

```cpp
#include <hip/hip_runtime.h>
#include <hip/hip_bf16.h>
#include <stdint.h>
#include <type_traits>

typedef __bf16 bf16x8 __attribute__((ext_vector_type(8)));
typedef __bf16 bf16x2 __attribute__((ext_vector_type(2)));
typedef short s16x4 __attribute__((ext_vector_type(4)));
typedef short s16x8 __attribute__((ext_vector_type(8)));
typedef float f32x2 __attribute__((ext_vector_type(2)));
typedef float f32x16 __attribute__((ext_vector_type(16)));
typedef unsigned u32x2 __attribute__((ext_vector_type(2)));
typedef unsigned u32x4 __attribute__((ext_vector_type(4)));
#define LAS __attribute__((address_space(3)))
#define DI __device__ __forceinline__

constexpr int NSTEP = 31;
constexpr int NSLOT = 4;
constexpr unsigned SLOT_BYTES = 32768;
constexpr unsigned SCR = NSLOT * SLOT_BYTES;

struct Sched { int slot[NSTEP]; int wait[NSTEP]; int issue_at[NSTEP]; };
constexpr int npieces(int t) { return t < 7 ? 10 : 8; }
constexpr int nextra(int t) { return 0; }
constexpr Sched make_sched() {
  Sched s{};
  for (int t = 0; t < NSTEP; ++t) {
    s.slot[t] = t % NSLOT;
    int last = (t == 0) ? 1 : (t + NSLOT - 2 < NSTEP - 1 ? t + NSLOT - 2 : NSTEP - 1);
    int w = nextra(t);
    for (int u = t + 1; u <= last; ++u) w += npieces(u) + nextra(u);
    s.wait[t] = w;
    s.issue_at[t] = (t >= 1 && t + NSLOT - 1 < NSTEP) ? t + NSLOT - 1 : -1;
  }
  return s;
}
constexpr Sched SCH = make_sched();

template <int B, int E, class F> DI void static_for(F&& f) {
  if constexpr (B < E) { f(std::integral_constant<int, B>{}); static_for<B + 1, E>(f); }
}

template <int N, int D, class L, class M> DI void pipe(L&& load, M&& mma) {
  bf16x8 fr[N];
  static_for<0, D>([&](auto ic) { fr[decltype(ic)::value] = load(ic); });
  __builtin_amdgcn_sched_barrier(0);
  static_for<0, N>([&](auto ic) {
    constexpr int i = decltype(ic)::value;
    if constexpr (i + D < N) fr[i + D] = load(std::integral_constant<int, i + D>{});
    mma(ic, fr[i]);
    __builtin_amdgcn_sched_barrier(0);
  });
}

DI void dma16(unsigned lds_addr, const char* sbase, unsigned voff) {
  asm volatile("s_mov_b32 m0, %0\n\ts_nop 0\n\tglobal_load_lds_dwordx4 %1, %2"
               :: "s"(lds_addr), "v"(voff), "s"(sbase) : "memory");
}
DI void dma16nt(unsigned lds_addr, const char* sbase, unsigned voff) {
  asm volatile("s_mov_b32 m0, %0\n\ts_nop 0\n\tglobal_load_lds_dwordx4 %1, %2 nt"
               :: "s"(lds_addr), "v"(voff), "s"(sbase) : "memory");
}
template <int N> DI void top_sync() {
  asm volatile("s_waitcnt vmcnt(%0) lgkmcnt(0)\n\ts_barrier" :: "n"(N) : "memory");
}
DI void lds_sync() { asm volatile("s_waitcnt lgkmcnt(0)\n\ts_barrier" ::: "memory"); }

DI unsigned pk2(float a, float b) {
  f32x2 v = {a, b};
  bf16x2 r = __builtin_convertvector(v, bf16x2);
  return __builtin_bit_cast(unsigned, r);
}
DI bf16x8 tr_frag(unsigned a0, unsigned a1) {
  s16x4 lo = __builtin_amdgcn_ds_read_tr16_b64_v4i16((LAS s16x4*)a0);
  s16x4 hi = __builtin_amdgcn_ds_read_tr16_b64_v4i16((LAS s16x4*)a1);
  s16x8 r = __builtin_shufflevector(lo, hi, 0, 1, 2, 3, 4, 5, 6, 7);
  return __builtin_bit_cast(bf16x8, r);
}
DI bf16x8 lds_b128(unsigned a) { return *(LAS const bf16x8*)a; }
DI float swap32_max(float v) {
  auto rr = __builtin_amdgcn_permlane32_swap(__float_as_uint(v), __float_as_uint(v), false, false);
  return fmaxf(__uint_as_float(rr[0]), __uint_as_float(rr[1]));
}
DI float swap32_add(float v) {
  auto rr = __builtin_amdgcn_permlane32_swap(__float_as_uint(v), __float_as_uint(v), false, false);
  return __uint_as_float(rr[0]) + __uint_as_float(rr[1]);
}

__global__ __launch_bounds__(512, 2) void attn_kernel(const char* __restrict__ x, const char* __restrict__ Kc,
                                                      const char* __restrict__ Vc, const char* __restrict__ wq,
                                                      const char* __restrict__ wo, char* __restrict__ slab) {
  __shared__ __attribute__((aligned(1024))) char smem[163840];
  const unsigned lds0 = (unsigned)(size_t)smem;
  const int tid = threadIdx.x, lane = tid & 63;
  const int wid8 = __builtin_amdgcn_readfirstlane(tid >> 6);
  const int wid = wid8 & 3;
  const int qb = wid & 1, role = wid >> 1;
  const int hi = lane >> 5, l31 = lane & 31;
  const int bid = blockIdx.x;
  const int xcd = bid & 7, idx = bid >> 3;
  const int qh = idx & 1, hl = (idx >> 1) & 1, b = idx >> 2, h = xcd * 2 + hl;

  if (wid8 >= 4) {
    const size_t kvo = (size_t)(b * 16 + h) * (512 * 512) + (size_t)((wid >> 1) * 256 + (wid & 1) * 16) * 512;
    const char* Kb = Kc + kvo;
    const char* Vb = Vc + kvo;
    const int rot = b;
    const char* Wqb = wq + (size_t)(wid * 16) * 8192 + h * 512;
    const char* Wob = wo + (size_t)(h * 256 + wid * 8) * 896;
    const char* Xb = x + (size_t)(b * 128 + qh * 64 + wid * 16) * 896;
    const unsigned ldsw = lds0 + wid * 8192;
    const int krot = idx % 7;
    auto issue = [&](auto sc) {
      constexpr int S = decltype(sc)::value;
      constexpr unsigned sl = SCH.slot[S] * SLOT_BYTES;
      const int kt = (S + krot >= 7) ? S + krot - 7 : S + krot;
      if constexpr (S < 7) {
        static_for<0, 2>([&](auto jc) {
          constexpr int j = decltype(jc)::value;
          unsigned voff = (lane >> 3) * 896 + (((lane & 7) ^ (lane >> 3) ^ j) << 4);
          dma16(lds0 + SCR + SCH.slot[S] * 8192 + (2 * wid + j) * 1024, Xb + (size_t)(8 * j) * 896 + 128 * kt, voff);
        });
      }
      static_for<0, 8>([&](auto jc) {
        constexpr int j = decltype(jc)::value;
        if constexpr (S < 7) {
          unsigned voff = hi * 8192 + ((l31 ^ ((2 * (j & 1) + hi) << 2)) << 4);
          dma16(ldsw + sl + j * 1024, Wqb + (size_t)(64 * kt + 2 * j) * 8192, voff);
        } else if constexpr (S < 15) {
          unsigned voff = hi * 512 + ((l31 ^ hi ^ (2 * j)) << 4);
          dma16nt(ldsw + sl + j * 1024, Kb + (size_t)(32 * ((S - 7 + rot) & 7) + 2 * j) * 512, voff);
        } else if constexpr (S < 23) {
          unsigned voff = hi * 512 + ((l31 ^ ((2 * (j & 1) + hi) << 2)) << 4);
          dma16nt(ldsw + sl + j * 1024, Vb + (size_t)(32 * ((S - 15 + rot) & 7) + 2 * j) * 512, voff);
        } else {
          unsigned g = lane ^ ((j & 3) << 2);
          g = (g >= 56) ? g - 8 : g;
          dma16(ldsw + sl + j * 1024, Wob + (size_t)(32 * (S - 23) + j) * 896, g << 4);
        }
      });
    };
    static_for<0, 2>([&](auto ic) { issue(ic); });
    static_for<0, NSTEP>([&](auto sc) {
      constexpr int S = decltype(sc)::value;
      top_sync<SCH.wait[S]>();
      if constexpr (S == 0) { issue(std::integral_constant<int, 2>{}); issue(std::integral_constant<int, 3>{}); }
      if constexpr (SCH.issue_at[S] >= 0) issue(std::integral_constant<int, SCH.issue_at[S]>{});
      if constexpr (S == 6 || S == 14) { asm volatile("s_barrier" ::: "memory"); }
      if constexpr (S == 22) { asm volatile("s_barrier" ::: "memory"); asm volatile("s_barrier" ::: "memory"); }
    });
    return;
  }

  auto top = [&](auto sc) { lds_sync(); };

  const int q4 = (lane & 15) >> 2, p = lane & 3, bb = (lane >> 4) & 1;
  unsigned trc[4];
#pragma unroll
  for (int blk = 0; blk < 4; ++blk) trc[blk] = ((16 * role + 4 * (blk ^ q4) + 2 * bb + (p >> 1)) << 4) + 8 * (p & 1);

  f32x16 acc[4];
#pragma unroll
  for (int i = 0; i < 4; ++i) acc[i] = f32x16{};
  const unsigned xr = lds0 + SCR + (4 * qb + (l31 >> 3)) * 1024 + (l31 & 7) * 128;
  const unsigned xsw = (l31 & 7) ^ ((l31 >> 3) & 1) ^ hi;
  static_for<0, 7>([&](auto tc) {
    constexpr int t = decltype(tc)::value;
    top(tc);
    constexpr unsigned sl = SCH.slot[t] * SLOT_BYTES;
    const unsigned rb = lds0 + sl + (8 * hi + q4) * 512;
    bf16x8 xf[4];
#pragma unroll
    for (int ks = 0; ks < 4; ++ks) xf[ks] = lds_b128(xr + SCH.slot[t] * 8192 + (((2 * ks) ^ xsw) << 4));
    pipe<16, 4>(
        [&](auto ic) { constexpr int i = decltype(ic)::value; constexpr int ks = i >> 2, blk = i & 3;
                       return tr_frag(rb + trc[blk] + (16 * ks) * 512, rb + trc[blk] + (16 * ks + 4) * 512); },
        [&](auto ic, bf16x8 a) { constexpr int i = decltype(ic)::value; constexpr int ks = i >> 2, blk = i & 3;
                       acc[blk] = __builtin_amdgcn_mfma_f32_32x32x16_bf16(a, xf[ks], acc[blk], 0, 0, 0); });
  });
  lds_sync();
  const unsigned qrow = lds0 + SCR + (32 * qb + l31) * 512;
#pragma unroll
  for (int blk = 0; blk < 4; ++blk)
#pragma unroll
    for (int g = 0; g < 4; ++g) {
      u32x2 w = {pk2(acc[blk][4 * g], acc[blk][4 * g + 1]), pk2(acc[blk][4 * g + 2], acc[blk][4 * g + 3])};
      *(LAS u32x2*)(qrow + (((16 * role + 4 * blk + g) ^ (l31 & 15)) << 4) + 8 * hi) = w;
    }

  f32x16 S[8];
  bf16x8 qf[16];
  float mx = -3.0e38f;
  static_for<0, 8>([&](auto tc) {
    constexpr int t = decltype(tc)::value;
    top(std::integral_constant<int, 7 + t>{});
    if constexpr (t == 0) {
#pragma unroll
      for (int s = 0; s < 16; ++s) qf[s] = lds_b128(qrow + (((2 * s + hi) ^ (l31 & 15)) << 4));
    }
    constexpr unsigned sl = SCH.slot[7 + t] * SLOT_BYTES;
    const unsigned kr = lds0 + sl + (32 * role + l31) * 512;
    f32x16 c = f32x16{};
    pipe<16, 4>(
        [&](auto ic) { constexpr int s = decltype(ic)::value; return lds_b128(kr + ((((2 * (s & 7)) + hi) ^ (l31 & 15)) << 4) + (s >> 3) * 256); },
        [&](auto ic, bf16x8 a) { constexpr int s = decltype(ic)::value;
                       c = __builtin_amdgcn_mfma_f32_32x32x16_bf16(a, qf[s], c, 0, 0, 0); });
    S[t] = c;
#pragma unroll
    for (int r = 0; r < 16; ++r) mx = fmaxf(mx, c[r]);
  });
  mx = swap32_max(mx);
  const unsigned xbuf = lds0 + SCR + 16384;
  *(LAS float*)(xbuf + wid * 256 + lane * 4) = mx;
  lds_sync();
  mx = fmaxf(mx, *(LAS const float*)(xbuf + (wid ^ 2) * 256 + lane * 4));

  constexpr float C = 0.0625f * 1.44269504088896340736f;
  const float mC = -mx * C;
  float lsum = 0.f;
  const unsigned pbuf = lds0 + SCR;
  bf16x8 pown[2];
  auto make_p = [&](const f32x16& s, int par) {
    float pe[16];
#pragma unroll
    for (int r = 0; r < 16; ++r) { pe[r] = __builtin_amdgcn_exp2f(fmaf(s[r], C, mC)); lsum += pe[r]; }
#pragma unroll
    for (int sp = 0; sp < 2; ++sp) {
      u32x4 w = {pk2(pe[8 * sp], pe[8 * sp + 1]), pk2(pe[8 * sp + 2], pe[8 * sp + 3]),
                 pk2(pe[8 * sp + 4], pe[8 * sp + 5]), pk2(pe[8 * sp + 6], pe[8 * sp + 7])};
      pown[sp] = __builtin_bit_cast(bf16x8, w);
      *(LAS u32x4*)(pbuf + par * 8192 + wid * 2048 + sp * 1024 + lane * 16) = w;
    }
  };
  make_p(S[0], 0);

  f32x16 o[4];
#pragma unroll
  for (int i = 0; i < 4; ++i) o[i] = f32x16{};
  static_for<0, 8>([&](auto tc) {
    constexpr int t = decltype(tc)::value;
    top(std::integral_constant<int, 15 + t>{});
    constexpr unsigned sl = SCH.slot[15 + t] * SLOT_BYTES;
    bf16x8 pf[2][2];
    pf[0][0] = pown[0]; pf[0][1] = pown[1];
    pf[1][0] = lds_b128(pbuf + (t & 1) * 8192 + (wid ^ 2) * 2048 + lane * 16);
    pf[1][1] = lds_b128(pbuf + (t & 1) * 8192 + (wid ^ 2) * 2048 + 1024 + lane * 16);
    const unsigned vr0 = lds0 + sl + (32 * role + 4 * hi + q4) * 512;
    const unsigned vr1 = lds0 + sl + (32 * (role ^ 1) + 4 * hi + q4) * 512;
    float pe[16];
    unsigned pk[8];
    pipe<16, 4>(
        [&](auto ic) { constexpr int i = decltype(ic)::value; constexpr int u = i >> 3, sp = (i >> 2) & 1, blk = i & 3;
                       const unsigned vr = u ? vr1 : vr0;
                       return tr_frag(vr + trc[blk] + (16 * sp) * 512, vr + trc[blk] + (16 * sp + 8) * 512); },
        [&](auto ic, bf16x8 a) { constexpr int i = decltype(ic)::value; constexpr int u = i >> 3, sp = (i >> 2) & 1, blk = i & 3;
                       o[blk] = __builtin_amdgcn_mfma_f32_32x32x16_bf16(a, pf[u][sp], o[blk], 0, 0, 0);
                       if constexpr (t < 7) {
                         pe[i] = __builtin_amdgcn_exp2f(fmaf(S[t + 1][i], C, mC)); lsum += pe[i];
                         if constexpr (i & 1) pk[i >> 1] = pk2(pe[i - 1], pe[i]);
                         if constexpr ((i & 7) == 7) {
                           constexpr int s4 = (i >> 3) * 4;
                           u32x4 w = {pk[s4], pk[s4 + 1], pk[s4 + 2], pk[s4 + 3]};
                           pown[i >> 3] = __builtin_bit_cast(bf16x8, w);
                           *(LAS u32x4*)(pbuf + ((t + 1) & 1) * 8192 + wid * 2048 + (i >> 3) * 1024 + lane * 16) = w;
                         }
                       } });
    if constexpr (t == 7) {
      float l2 = swap32_add(lsum);
      *(LAS float*)(xbuf + wid * 256 + lane * 4) = l2;
      lsum = l2;
    }
  });

  bf16x8 ofo[4][2], ofp[4][2];
  const unsigned obuf = lds0 + SCR + 0;
  f32x16 acc2[7];
#pragma unroll
  for (int i = 0; i < 7; ++i) acc2[i] = f32x16{};
  lds_sync();
  {
    float lt = lsum + *(LAS const float*)(xbuf + (wid ^ 2) * 256 + lane * 4);
    float inv = 1.0f / lt;
#pragma unroll
    for (int blk = 0; blk < 4; ++blk)
#pragma unroll
      for (int sp = 0; sp < 2; ++sp) {
        u32x4 w = {pk2(o[blk][8 * sp] * inv, o[blk][8 * sp + 1] * inv), pk2(o[blk][8 * sp + 2] * inv, o[blk][8 * sp + 3] * inv),
                   pk2(o[blk][8 * sp + 4] * inv, o[blk][8 * sp + 5] * inv), pk2(o[blk][8 * sp + 6] * inv, o[blk][8 * sp + 7] * inv)};
        ofo[blk][sp] = __builtin_bit_cast(bf16x8, w);
      }
  }
  lds_sync();
#pragma unroll
  for (int blk = 0; blk < 4; ++blk)
#pragma unroll
    for (int sp = 0; sp < 2; ++sp)
      *(LAS u32x4*)(obuf + wid * 8192 + (blk * 2 + sp) * 1024 + lane * 16) = __builtin_bit_cast(u32x4, ofo[blk][sp]);

  unsigned trw[7];
#pragma unroll
  for (int nb = 0; nb < 7; ++nb) trw[nb] = ((4 * ((7 * role + nb) ^ q4) + 2 * bb + (p >> 1)) << 4) + 8 * (p & 1);
  static_for<0, 8>([&](auto tc) {
    constexpr int t = decltype(tc)::value;
    top(std::integral_constant<int, 23 + t>{});
    if constexpr (t == 0) {
#pragma unroll
      for (int blk = 0; blk < 4; ++blk)
#pragma unroll
        for (int sp = 0; sp < 2; ++sp)
          ofp[blk][sp] = lds_b128(obuf + (wid ^ 2) * 8192 + (blk * 2 + sp) * 1024 + lane * 16);
    }
    constexpr unsigned sl = SCH.slot[23 + t] * SLOT_BYTES;
    const unsigned wr = lds0 + sl + (4 * hi + q4) * 1024;
    const bf16x8 bf0 = ((t >> 2) == role) ? ofo[t & 3][0] : ofp[t & 3][0];
    const bf16x8 bf1 = ((t >> 2) == role) ? ofo[t & 3][1] : ofp[t & 3][1];
    pipe<14, 4>(
        [&](auto ic) { constexpr int i = decltype(ic)::value; constexpr int sp = i / 7, nb = i % 7;
                       return tr_frag(wr + trw[nb] + (16 * sp) * 1024, wr + trw[nb] + (16 * sp + 8) * 1024); },
        [&](auto ic, bf16x8 a) { constexpr int i = decltype(ic)::value; constexpr int sp = i / 7, nb = i % 7;
                       acc2[nb] = __builtin_amdgcn_mfma_f32_32x32x16_bf16(a, sp ? bf1 : bf0, acc2[nb], 0, 0, 0); });
  });

  char* sp_ = slab + ((size_t)(((h * 8 + b) * 2 + qh) * 4 + wid) * 14) * 1024 + lane * 16;
#pragma unroll
  for (int nb = 0; nb < 7; ++nb)
#pragma unroll
    for (int g2 = 0; g2 < 2; ++g2) {
      u32x4 w = {pk2(acc2[nb][8 * g2], acc2[nb][8 * g2 + 1]), pk2(acc2[nb][8 * g2 + 2], acc2[nb][8 * g2 + 3]),
                 pk2(acc2[nb][8 * g2 + 4], acc2[nb][8 * g2 + 5]), pk2(acc2[nb][8 * g2 + 6], acc2[nb][8 * g2 + 7])};
      *(u32x4*)(sp_ + (nb * 2 + g2) * 1024) = w;
    }
}

__global__ __launch_bounds__(256) void reduce_kernel(const u32x4* __restrict__ slab, char* __restrict__ out) {
  const int i = blockIdx.x * 256 + threadIdx.x;
  float s[8];
#pragma unroll
  for (int k = 0; k < 8; ++k) s[k] = 0.f;
  u32x4 v[16];
#pragma unroll
  for (int h = 0; h < 16; ++h) v[h] = slab[(size_t)h * 57344 + i];
#pragma unroll
  for (int h = 0; h < 16; ++h)
#pragma unroll
    for (int k = 0; k < 4; ++k) {
      s[2 * k] += __uint_as_float(v[h][k] << 16);
      s[2 * k + 1] += __uint_as_float(v[h][k] & 0xffff0000u);
    }
  const int lane = i & 63, g2 = (i >> 6) & 1;
  int rest = i >> 7;
  const int nb = rest % 7; rest /= 7;
  const int wid = rest & 3, qh = (rest >> 2) & 1, b = rest >> 3;
  const int row = b * 128 + qh * 64 + (wid & 1) * 32 + (lane & 31);
  const int n0 = 32 * (7 * (wid >> 1) + nb) + 16 * g2 + 4 * (lane >> 5);
  char* o = out + ((size_t)row * 448 + n0) * 2;
  u32x2 w0 = {pk2(s[0], s[1]), pk2(s[2], s[3])};
  u32x2 w1 = {pk2(s[4], s[5]), pk2(s[6], s[7])};
  *(u32x2*)o = w0;
  *(u32x2*)(o + 16) = w1;
}

extern "C" void kernel_launch(void* const* d_in, const int* in_sizes, int n_in, void* d_out, int out_size,
                              void* d_ws, size_t ws_size, hipStream_t stream) {
  const char* x = (const char*)d_in[0];
  const char* k = (const char*)d_in[1];
  const char* v = (const char*)d_in[2];
  const char* wq = (const char*)d_in[3];
  const char* wo = (const char*)d_in[4];
  char* slab = (char*)d_ws;
  attn_kernel<<<256, 512, 0, stream>>>(x, k, v, wq, wo, slab);
  reduce_kernel<<<224, 256, 0, stream>>>((const u32x4*)slab, (char*)d_out);
}
```

```cpp
#include <hip/hip_runtime.h>
#include <hip/hip_bf16.h>
#include <stdint.h>
#include <type_traits>

typedef __bf16 bf16x8 __attribute__((ext_vector_type(8)));
typedef __bf16 bf16x2 __attribute__((ext_vector_type(2)));
typedef short s16x4 __attribute__((ext_vector_type(4)));
typedef short s16x8 __attribute__((ext_vector_type(8)));
typedef float f32x2 __attribute__((ext_vector_type(2)));
typedef float f32x16 __attribute__((ext_vector_type(16)));
typedef unsigned u32x2 __attribute__((ext_vector_type(2)));
typedef unsigned u32x4 __attribute__((ext_vector_type(4)));
#define LAS __attribute__((address_space(3)))
#define DI __device__ __forceinline__

constexpr int NSTEP = 31;
constexpr int NSLOT = 4;
constexpr unsigned SLOT_BYTES = 32768;
constexpr unsigned SCR = NSLOT * SLOT_BYTES;

struct Sched { int slot[NSTEP]; int waitB[NSTEP]; int waitW[NSTEP]; int kv0[NSTEP + 1]; int kv1[NSTEP + 1]; };
constexpr int NBUF = 7;
constexpr int WR_AHEAD = 1;
constexpr int clampc(int v) { return v > 63 ? 63 : v; }
constexpr int budget(int t) { return t < 3 ? 0 : 8; }
constexpr Sched make_sched() {
  Sched s{};
  int cum = 0;
  int endT[NSTEP] = {};
  int n = 0;
  auto W = [&](int t) { cum += 10; endT[t] = cum; };
  auto O = [&](int t) { cum += 8; endT[t] = cum; };
  auto KV = [&](int slot, int cnt, int maxtile) {
    s.kv0[slot] = n;
    for (int c = 0; c < cnt && n < 128 && (n >> 3) <= maxtile; ++c) { ++cum; endT[7 + (n >> 3)] = cum; ++n; }
    s.kv1[slot] = n;
  };
  W(0); W(1); KV(NSTEP, budget(-1), NBUF - 1);
  for (int t = 0; t < NSTEP; ++t) {
    s.slot[t] = t % NSLOT;
    s.waitB[t] = (t < 7 || t >= 23) ? clampc(cum - endT[t]) : 63;
    s.waitW[t] = 63;
    if (t == 0) { W(2); W(3); }
    if (t >= 1 && t <= 3) W(t + 3);
    const int wt = t + WR_AHEAD;
    if (wt >= 7 && wt < 23) s.waitW[t] = clampc(cum - endT[wt]);
    int maxtile = wt - 7 + NBUF; if (maxtile < NBUF - 1) maxtile = NBUF - 1;
    KV(t, budget(t), maxtile);
    if (t + 3 >= 23 && t + 3 < NSTEP) O(t + 3);
  }
  return s;
}
constexpr Sched SCH = make_sched();

template <int B, int E, class F> DI void static_for(F&& f) {
  if constexpr (B < E) { f(std::integral_constant<int, B>{}); static_for<B + 1, E>(f); }
}

template <int N, int D, class L, class M> DI void pipe(L&& load, M&& mma) {
  bf16x8 fr[N];
  static_for<0, D>([&](auto ic) { fr[decltype(ic)::value] = load(ic); });
  __builtin_amdgcn_sched_barrier(0);
  static_for<0, N>([&](auto ic) {
    constexpr int i = decltype(ic)::value;
    if constexpr (i + D < N) fr[i + D] = load(std::integral_constant<int, i + D>{});
    mma(ic, fr[i]);
    __builtin_amdgcn_sched_barrier(0);
  });
}

DI void dma16(unsigned lds_addr, const char* sbase, unsigned voff) {
  asm volatile("s_mov_b32 m0, %0\n\ts_nop 0\n\tglobal_load_lds_dwordx4 %1, %2"
               :: "s"(lds_addr), "v"(voff), "s"(sbase) : "memory");
}
DI void dma16nt(unsigned lds_addr, const char* sbase, unsigned voff) {
  asm volatile("s_mov_b32 m0, %0\n\ts_nop 0\n\tglobal_load_lds_dwordx4 %1, %2 nt"
               :: "s"(lds_addr), "v"(voff), "s"(sbase) : "memory");
}
DI void ld16nt(u32x4& d, const char* sbase, unsigned voff) {
  asm volatile("global_load_dwordx4 %0, %1, %2 nt" : "=v"(d) : "v"(voff), "s"(sbase) : "memory");
}
template <unsigned OFF> DI void st16(unsigned addr, const u32x4& d) {
  if constexpr (OFF < 65536) asm volatile("ds_write_b128 %0, %1 offset:%2" :: "v"(addr), "v"(d), "n"(OFF) : "memory");
  else asm volatile("ds_write_b128 %0, %1 offset:%2" :: "v"(addr + 65536u), "v"(d), "n"(OFF - 65536u) : "memory");
}
template <int N> DI void wait_vm() { asm volatile("s_waitcnt vmcnt(%0)" :: "n"(N) : "memory"); }
template <int N> DI void top_sync() {
  asm volatile("s_waitcnt vmcnt(%0) lgkmcnt(0)\n\ts_barrier" :: "n"(N) : "memory");
}
DI void lds_sync() { asm volatile("s_waitcnt lgkmcnt(0)\n\ts_barrier" ::: "memory"); }

DI unsigned pk2(float a, float b) {
  f32x2 v = {a, b};
  bf16x2 r = __builtin_convertvector(v, bf16x2);
  return __builtin_bit_cast(unsigned, r);
}
DI bf16x8 tr_frag(unsigned a0, unsigned a1) {
  s16x4 lo = __builtin_amdgcn_ds_read_tr16_b64_v4i16((LAS s16x4*)a0);
  s16x4 hi = __builtin_amdgcn_ds_read_tr16_b64_v4i16((LAS s16x4*)a1);
  s16x8 r = __builtin_shufflevector(lo, hi, 0, 1, 2, 3, 4, 5, 6, 7);
  return __builtin_bit_cast(bf16x8, r);
}
DI bf16x8 lds_b128(unsigned a) { return *(LAS const bf16x8*)a; }
DI float swap32_max(float v) {
  auto rr = __builtin_amdgcn_permlane32_swap(__float_as_uint(v), __float_as_uint(v), false, false);
  return fmaxf(__uint_as_float(rr[0]), __uint_as_float(rr[1]));
}
DI float swap32_add(float v) {
  auto rr = __builtin_amdgcn_permlane32_swap(__float_as_uint(v), __float_as_uint(v), false, false);
  return __uint_as_float(rr[0]) + __uint_as_float(rr[1]);
}

__global__ __launch_bounds__(512, 2) void attn_kernel(const char* __restrict__ x, const char* __restrict__ Kc,
                                                      const char* __restrict__ Vc, const char* __restrict__ wq,
                                                      const char* __restrict__ wo, char* __restrict__ slab) {
  __shared__ __attribute__((aligned(1024))) char smem[163840];
  const unsigned lds0 = (unsigned)(size_t)smem;
  const int tid = threadIdx.x, lane = tid & 63;
  const int wid8 = __builtin_amdgcn_readfirstlane(tid >> 6);
  const int wid = wid8 & 3;
  const int qb = wid & 1, role = wid >> 1;
  const int hi = lane >> 5, l31 = lane & 31;
  const int bid = blockIdx.x;
  const int xcd = bid & 7, idx = bid >> 3;
  const int qh = idx & 1, hl = (idx >> 1) & 1, b = idx >> 2, h = xcd * 2 + hl;

  if (wid8 >= 4) {
    const size_t kvo = (size_t)(b * 16 + h) * (512 * 512) + (size_t)((wid >> 1) * 256 + (wid & 1) * 16) * 512;
    const char* Kb = Kc + kvo;
    const char* Vb = Vc + kvo;
    const int rot = b;
    const char* Wqb = wq + (size_t)(wid * 16) * 8192 + h * 512;
    const char* Wob = wo + (size_t)(h * 256 + wid * 8) * 896;
    const char* Xb = x + (size_t)(b * 128 + qh * 64 + wid * 16) * 896;
    const unsigned ldsw = lds0 + wid * 8192;
    const int krot = idx % 7;
    auto issue = [&](auto sc) {
      constexpr int S = decltype(sc)::value;
      constexpr unsigned sl = SCH.slot[S] * SLOT_BYTES;
      const int kt = (S + krot >= 7) ? S + krot - 7 : S + krot;
      if constexpr (S < 7) {
        static_for<0, 2>([&](auto jc) {
          constexpr int j = decltype(jc)::value;
          unsigned voff = (lane >> 3) * 896 + (((lane & 7) ^ (lane >> 3) ^ j) << 4);
          dma16(lds0 + SCR + SCH.slot[S] * 8192 + (2 * wid + j) * 1024, Xb + (size_t)(8 * j) * 896 + 128 * kt, voff);
        });
      }
      static_for<0, 8>([&](auto jc) {
        constexpr int j = decltype(jc)::value;
        if constexpr (S < 7) {
          unsigned voff = hi * 8192 + ((l31 ^ ((2 * (j & 1) + hi) << 2)) << 4);
          dma16(ldsw + sl + j * 1024, Wqb + (size_t)(64 * kt + 2 * j) * 8192, voff);
        } else {
          unsigned g = lane ^ ((j & 3) << 2);
          g = (g >= 56) ? g - 8 : g;
          dma16(ldsw + sl + j * 1024, Wob + (size_t)(32 * (S - 23) + j) * 896, g << 4);
        }
      });
    };
    u32x4 buf[NBUF][8];
    auto kv_load = [&](auto ac, auto bc) {
      static_for<decltype(ac)::value, decltype(bc)::value>([&](auto nc) {
        constexpr int I = decltype(nc)::value >> 3, j = decltype(nc)::value & 7;
        if constexpr (I < 8) {
          unsigned voff = hi * 512 + ((l31 ^ hi ^ (2 * j)) << 4);
          ld16nt(buf[I % NBUF][j], Kb + (size_t)(32 * ((I + rot) & 7) + 2 * j) * 512, voff);
        } else {
          unsigned voff = hi * 512 + ((l31 ^ ((2 * (j & 1) + hi) << 2)) << 4);
          ld16nt(buf[I % NBUF][j], Vb + (size_t)(32 * ((I - 8 + rot) & 7) + 2 * j) * 512, voff);
        }
      });
    };
    const unsigned ldsl = ldsw + lane * 16;
    auto kv_write = [&](auto ic) {
      constexpr int I = decltype(ic)::value;
      constexpr unsigned sl = SCH.slot[7 + I] * SLOT_BYTES;
      static_for<0, 8>([&](auto jc) {
        constexpr int j = decltype(jc)::value;
        st16<sl + j * 1024>(ldsl, buf[I % NBUF][j]);
      });
    };
#define IC(v) std::integral_constant<int, (v)>{}
    issue(IC(0)); issue(IC(1)); kv_load(IC(SCH.kv0[NSTEP]), IC(SCH.kv1[NSTEP]));
    static_for<0, NSTEP>([&](auto sc) {
      constexpr int S = decltype(sc)::value;
      top_sync<SCH.waitB[S]>();
      if constexpr (S == 0) { issue(IC(2)); issue(IC(3)); }
      if constexpr (S >= 1 && S <= 3) issue(IC(S + 3));
      constexpr int WT = S + WR_AHEAD;
      if constexpr (WT >= 7 && WT < 23) {
        wait_vm<SCH.waitW[S]>();
        kv_write(IC(WT - 7));
      }
      kv_load(IC(SCH.kv0[S]), IC(SCH.kv1[S]));
      if constexpr (S + 3 >= 23 && S + 3 < NSTEP) issue(IC(S + 3));
      if constexpr (S == 6 || S == 14) { asm volatile("s_barrier" ::: "memory"); }
      if constexpr (S == 22) { asm volatile("s_barrier" ::: "memory"); asm volatile("s_barrier" ::: "memory"); }
    });
    return;
  }

  auto top = [&](auto sc) { lds_sync(); };

  const int q4 = (lane & 15) >> 2, p = lane & 3, bb = (lane >> 4) & 1;
  unsigned trc[4];
#pragma unroll
  for (int blk = 0; blk < 4; ++blk) trc[blk] = ((16 * role + 4 * (blk ^ q4) + 2 * bb + (p >> 1)) << 4) + 8 * (p & 1);

  f32x16 acc[4];
#pragma unroll
  for (int i = 0; i < 4; ++i) acc[i] = f32x16{};
  const unsigned xr = lds0 + SCR + (4 * qb + (l31 >> 3)) * 1024 + (l31 & 7) * 128;
  const unsigned xsw = (l31 & 7) ^ ((l31 >> 3) & 1) ^ hi;
  static_for<0, 7>([&](auto tc) {
    constexpr int t = decltype(tc)::value;
    top(tc);
    constexpr unsigned sl = SCH.slot[t] * SLOT_BYTES;
    const unsigned rb = lds0 + sl + (8 * hi + q4) * 512;
    bf16x8 xf[4];
#pragma unroll
    for (int ks = 0; ks < 4; ++ks) xf[ks] = lds_b128(xr + SCH.slot[t] * 8192 + (((2 * ks) ^ xsw) << 4));
    pipe<16, 4>(
        [&](auto ic) { constexpr int i = decltype(ic)::value; constexpr int ks = i >> 2, blk = i & 3;
                       return tr_frag(rb + trc[blk] + (16 * ks) * 512, rb + trc[blk] + (16 * ks + 4) * 512); },
        [&](auto ic, bf16x8 a) { constexpr int i = decltype(ic)::value; constexpr int ks = i >> 2, blk = i & 3;
                       acc[blk] = __builtin_amdgcn_mfma_f32_32x32x16_bf16(a, xf[ks], acc[blk], 0, 0, 0); });
  });
  lds_sync();
  const unsigned qrow = lds0 + SCR + (32 * qb + l31) * 512;
#pragma unroll
  for (int blk = 0; blk < 4; ++blk)
#pragma unroll
    for (int g = 0; g < 4; ++g) {
      u32x2 w = {pk2(acc[blk][4 * g], acc[blk][4 * g + 1]), pk2(acc[blk][4 * g + 2], acc[blk][4 * g + 3])};
      *(LAS u32x2*)(qrow + (((16 * role + 4 * blk + g) ^ (l31 & 15)) << 4) + 8 * hi) = w;
    }

  f32x16 S[8];
  bf16x8 qf[16];
  float mx = -3.0e38f;
  static_for<0, 8>([&](auto tc) {
    constexpr int t = decltype(tc)::value;
    top(std::integral_constant<int, 7 + t>{});
    if constexpr (t == 0) {
#pragma unroll
      for (int s = 0; s < 16; ++s) qf[s] = lds_b128(qrow + (((2 * s + hi) ^ (l31 & 15)) << 4));
    }
    constexpr unsigned sl = SCH.slot[7 + t] * SLOT_BYTES;
    const unsigned kr = lds0 + sl + (32 * role + l31) * 512;
    f32x16 c = f32x16{};
    pipe<16, 4>(
        [&](auto ic) { constexpr int s = decltype(ic)::value; return lds_b128(kr + ((((2 * (s & 7)) + hi) ^ (l31 & 15)) << 4) + (s >> 3) * 256); },
        [&](auto ic, bf16x8 a) { constexpr int s = decltype(ic)::value;
                       c = __builtin_amdgcn_mfma_f32_32x32x16_bf16(a, qf[s], c, 0, 0, 0); });
    S[t] = c;
#pragma unroll
    for (int r = 0; r < 16; ++r) mx = fmaxf(mx, c[r]);
  });
  mx = swap32_max(mx);
  const unsigned xbuf = lds0 + SCR + 16384;
  *(LAS float*)(xbuf + wid * 256 + lane * 4) = mx;
  lds_sync();
  mx = fmaxf(mx, *(LAS const float*)(xbuf + (wid ^ 2) * 256 + lane * 4));

  constexpr float C = 0.0625f * 1.44269504088896340736f;
  const float mC = -mx * C;
  float lsum = 0.f;
  const unsigned pbuf = lds0 + SCR;
  bf16x8 pown[2];
  auto make_p = [&](const f32x16& s, int par) {
    float pe[16];
#pragma unroll
    for (int r = 0; r < 16; ++r) { pe[r] = __builtin_amdgcn_exp2f(fmaf(s[r], C, mC)); lsum += pe[r]; }
#pragma unroll
    for (int sp = 0; sp < 2; ++sp) {
      u32x4 w = {pk2(pe[8 * sp], pe[8 * sp + 1]), pk2(pe[8 * sp + 2], pe[8 * sp + 3]),
                 pk2(pe[8 * sp + 4], pe[8 * sp + 5]), pk2(pe[8 * sp + 6], pe[8 * sp + 7])};
      pown[sp] = __builtin_bit_cast(bf16x8, w);
      *(LAS u32x4*)(pbuf + par * 8192 + wid * 2048 + sp * 1024 + lane * 16) = w;
    }
  };
  make_p(S[0], 0);

  f32x16 o[4];
#pragma unroll
  for (int i = 0; i < 4; ++i) o[i] = f32x16{};
  static_for<0, 8>([&](auto tc) {
    constexpr int t = decltype(tc)::value;
    top(std::integral_constant<int, 15 + t>{});
    constexpr unsigned sl = SCH.slot[15 + t] * SLOT_BYTES;
    bf16x8 pf[2][2];
    pf[0][0] = pown[0]; pf[0][1] = pown[1];
    pf[1][0] = lds_b128(pbuf + (t & 1) * 8192 + (wid ^ 2) * 2048 + lane * 16);
    pf[1][1] = lds_b128(pbuf + (t & 1) * 8192 + (wid ^ 2) * 2048 + 1024 + lane * 16);
    const unsigned vr0 = lds0 + sl + (32 * role + 4 * hi + q4) * 512;
    const unsigned vr1 = lds0 + sl + (32 * (role ^ 1) + 4 * hi + q4) * 512;
    float pe[16];
    unsigned pk[8];
    pipe<16, 4>(
        [&](auto ic) { constexpr int i = decltype(ic)::value; constexpr int u = i >> 3, sp = (i >> 2) & 1, blk = i & 3;
                       const unsigned vr = u ? vr1 : vr0;
                       return tr_frag(vr + trc[blk] + (16 * sp) * 512, vr + trc[blk] + (16 * sp + 8) * 512); },
        [&](auto ic, bf16x8 a) { constexpr int i = decltype(ic)::value; constexpr int u = i >> 3, sp = (i >> 2) & 1, blk = i & 3;
                       o[blk] = __builtin_amdgcn_mfma_f32_32x32x16_bf16(a, pf[u][sp], o[blk], 0, 0, 0);
                       if constexpr (t < 7) {
                         pe[i] = __builtin_amdgcn_exp2f(fmaf(S[t + 1][i], C, mC)); lsum += pe[i];
                         if constexpr (i & 1) pk[i >> 1] = pk2(pe[i - 1], pe[i]);
                         if constexpr ((i & 7) == 7) {
                           constexpr int s4 = (i >> 3) * 4;
                           u32x4 w = {pk[s4], pk[s4 + 1], pk[s4 + 2], pk[s4 + 3]};
                           pown[i >> 3] = __builtin_bit_cast(bf16x8, w);
                           *(LAS u32x4*)(pbuf + ((t + 1) & 1) * 8192 + wid * 2048 + (i >> 3) * 1024 + lane * 16) = w;
                         }
                       } });
    if constexpr (t == 7) {
      float l2 = swap32_add(lsum);
      *(LAS float*)(xbuf + wid * 256 + lane * 4) = l2;
      lsum = l2;
    }
  });

  bf16x8 ofo[4][2], ofp[4][2];
  const unsigned obuf = lds0 + SCR + 0;
  f32x16 acc2[7];
#pragma unroll
  for (int i = 0; i < 7; ++i) acc2[i] = f32x16{};
  lds_sync();
  {
    float lt = lsum + *(LAS const float*)(xbuf + (wid ^ 2) * 256 + lane * 4);
    float inv = 1.0f / lt;
#pragma unroll
    for (int blk = 0; blk < 4; ++blk)
#pragma unroll
      for (int sp = 0; sp < 2; ++sp) {
        u32x4 w = {pk2(o[blk][8 * sp] * inv, o[blk][8 * sp + 1] * inv), pk2(o[blk][8 * sp + 2] * inv, o[blk][8 * sp + 3] * inv),
                   pk2(o[blk][8 * sp + 4] * inv, o[blk][8 * sp + 5] * inv), pk2(o[blk][8 * sp + 6] * inv, o[blk][8 * sp + 7] * inv)};
        ofo[blk][sp] = __builtin_bit_cast(bf16x8, w);
      }
  }
  lds_sync();
#pragma unroll
  for (int blk = 0; blk < 4; ++blk)
#pragma unroll
    for (int sp = 0; sp < 2; ++sp)
      *(LAS u32x4*)(obuf + wid * 8192 + (blk * 2 + sp) * 1024 + lane * 16) = __builtin_bit_cast(u32x4, ofo[blk][sp]);

  unsigned trw[7];
#pragma unroll
  for (int nb = 0; nb < 7; ++nb) trw[nb] = ((4 * ((7 * role + nb) ^ q4) + 2 * bb + (p >> 1)) << 4) + 8 * (p & 1);
  static_for<0, 8>([&](auto tc) {
    constexpr int t = decltype(tc)::value;
    top(std::integral_constant<int, 23 + t>{});
    if constexpr (t == 0) {
#pragma unroll
      for (int blk = 0; blk < 4; ++blk)
#pragma unroll
        for (int sp = 0; sp < 2; ++sp)
          ofp[blk][sp] = lds_b128(obuf + (wid ^ 2) * 8192 + (blk * 2 + sp) * 1024 + lane * 16);
    }
    constexpr unsigned sl = SCH.slot[23 + t] * SLOT_BYTES;
    const unsigned wr = lds0 + sl + (4 * hi + q4) * 1024;
    const bf16x8 bf0 = ((t >> 2) == role) ? ofo[t & 3][0] : ofp[t & 3][0];
    const bf16x8 bf1 = ((t >> 2) == role) ? ofo[t & 3][1] : ofp[t & 3][1];
    pipe<14, 4>(
        [&](auto ic) { constexpr int i = decltype(ic)::value; constexpr int sp = i / 7, nb = i % 7;
                       return tr_frag(wr + trw[nb] + (16 * sp) * 1024, wr + trw[nb] + (16 * sp + 8) * 1024); },
        [&](auto ic, bf16x8 a) { constexpr int i = decltype(ic)::value; constexpr int sp = i / 7, nb = i % 7;
                       acc2[nb] = __builtin_amdgcn_mfma_f32_32x32x16_bf16(a, sp ? bf1 : bf0, acc2[nb], 0, 0, 0); });
  });

  char* sp_ = slab + ((size_t)(((h * 8 + b) * 2 + qh) * 4 + wid) * 14) * 1024 + lane * 16;
#pragma unroll
  for (int nb = 0; nb < 7; ++nb)
#pragma unroll
    for (int g2 = 0; g2 < 2; ++g2) {
      u32x4 w = {pk2(acc2[nb][8 * g2], acc2[nb][8 * g2 + 1]), pk2(acc2[nb][8 * g2 + 2], acc2[nb][8 * g2 + 3]),
                 pk2(acc2[nb][8 * g2 + 4], acc2[nb][8 * g2 + 5]), pk2(acc2[nb][8 * g2 + 6], acc2[nb][8 * g2 + 7])};
      __builtin_nontemporal_store(w, (u32x4*)(sp_ + (nb * 2 + g2) * 1024));
    }
}

__global__ __launch_bounds__(256) void reduce_kernel(const u32x4* __restrict__ slab, char* __restrict__ out) {
  const int i = blockIdx.x * 256 + threadIdx.x;
  float s[8];
#pragma unroll
  for (int k = 0; k < 8; ++k) s[k] = 0.f;
  u32x4 v[16];
#pragma unroll
  for (int h = 0; h < 16; ++h) v[h] = __builtin_nontemporal_load(&slab[(size_t)h * 57344 + i]);
#pragma unroll
  for (int h = 0; h < 16; ++h)
#pragma unroll
    for (int k = 0; k < 4; ++k) {
      s[2 * k] += __uint_as_float(v[h][k] << 16);
      s[2 * k + 1] += __uint_as_float(v[h][k] & 0xffff0000u);
    }
  const int lane = i & 63, g2 = (i >> 6) & 1;
  int rest = i >> 7;
  const int nb = rest % 7; rest /= 7;
  const int wid = rest & 3, qh = (rest >> 2) & 1, b = rest >> 3;
  const int row = b * 128 + qh * 64 + (wid & 1) * 32 + (lane & 31);
  const int n0 = 32 * (7 * (wid >> 1) + nb) + 16 * g2 + 4 * (lane >> 5);
  char* o = out + ((size_t)row * 448 + n0) * 2;
  u32x2 w0 = {pk2(s[0], s[1]), pk2(s[2], s[3])};
  u32x2 w1 = {pk2(s[4], s[5]), pk2(s[6], s[7])};
  *(u32x2*)o = w0;
  *(u32x2*)(o + 16) = w1;
}

extern "C" void kernel_launch(void* const* d_in, const int* in_sizes, int n_in, void* d_out, int out_size,
                              void* d_ws, size_t ws_size, hipStream_t stream) {
  const char* x = (const char*)d_in[0];
  const char* k = (const char*)d_in[1];
  const char* v = (const char*)d_in[2];
  const char* wq = (const char*)d_in[3];
  const char* wo = (const char*)d_in[4];
  char* slab = (char*)d_ws;
  attn_kernel<<<256, 512, 0, stream>>>(x, k, v, wq, wo, slab);
  reduce_kernel<<<224, 256, 0, stream>>>((const u32x4*)slab, (char*)d_out);
}
```

```cpp
#include <hip/hip_runtime.h>
#include <hip/hip_bf16.h>
#include <stdint.h>
#include <type_traits>

typedef __bf16 bf16x8 __attribute__((ext_vector_type(8)));
typedef __bf16 bf16x2 __attribute__((ext_vector_type(2)));
typedef short s16x4 __attribute__((ext_vector_type(4)));
typedef short s16x8 __attribute__((ext_vector_type(8)));
typedef float f32x2 __attribute__((ext_vector_type(2)));
typedef float f32x16 __attribute__((ext_vector_type(16)));
typedef unsigned u32x2 __attribute__((ext_vector_type(2)));
typedef unsigned u32x4 __attribute__((ext_vector_type(4)));
#define LAS __attribute__((address_space(3)))
#define DI __device__ __forceinline__

constexpr int NSTEP = 31;
constexpr int NSLOT = 4;
constexpr unsigned SLOT_BYTES = 32768;
constexpr unsigned SCR = NSLOT * SLOT_BYTES;

struct Sched { int slot[NSTEP]; int wait[NSTEP]; int issue_at[NSTEP]; };
constexpr int npieces(int t) { return t < 7 ? 10 : 8; }
constexpr int nextra(int t) { return 0; }
constexpr Sched make_sched() {
  Sched s{};
  for (int t = 0; t < NSTEP; ++t) {
    s.slot[t] = t % NSLOT;
    int last = (t == 0) ? 1 : (t + NSLOT - 2 < NSTEP - 1 ? t + NSLOT - 2 : NSTEP - 1);
    int w = nextra(t);
    for (int u = t + 1; u <= last; ++u) w += npieces(u) + nextra(u);
    s.wait[t] = w;
    s.issue_at[t] = (t >= 1 && t + NSLOT - 1 < NSTEP) ? t + NSLOT - 1 : -1;
  }
  return s;
}
constexpr Sched SCH = make_sched();

template <int B, int E, class F> DI void static_for(F&& f) {
  if constexpr (B < E) { f(std::integral_constant<int, B>{}); static_for<B + 1, E>(f); }
}

template <int N, int D, class L, class M> DI void pipe(L&& load, M&& mma) {
  bf16x8 fr[N];
  static_for<0, D>([&](auto ic) { fr[decltype(ic)::value] = load(ic); });
  __builtin_amdgcn_sched_barrier(0);
  static_for<0, N>([&](auto ic) {
    constexpr int i = decltype(ic)::value;
    if constexpr (i + D < N) fr[i + D] = load(std::integral_constant<int, i + D>{});
    mma(ic, fr[i]);
    __builtin_amdgcn_sched_barrier(0);
  });
}

DI void dma16(unsigned lds_addr, const char* sbase, unsigned voff) {
  asm volatile("s_mov_b32 m0, %0\n\ts_nop 0\n\tglobal_load_lds_dwordx4 %1, %2"
               :: "s"(lds_addr), "v"(voff), "s"(sbase) : "memory");
}
DI void dma16nt(unsigned lds_addr, const char* sbase, unsigned voff) {
  asm volatile("s_mov_b32 m0, %0\n\ts_nop 0\n\tglobal_load_lds_dwordx4 %1, %2 nt"
               :: "s"(lds_addr), "v"(voff), "s"(sbase) : "memory");
}
template <int N> DI void top_sync() {
  asm volatile("s_waitcnt vmcnt(%0) lgkmcnt(0)\n\ts_barrier" :: "n"(N) : "memory");
}
DI void lds_sync() { asm volatile("s_waitcnt lgkmcnt(0)\n\ts_barrier" ::: "memory"); }

DI unsigned pk2(float a, float b) {
  f32x2 v = {a, b};
  bf16x2 r = __builtin_convertvector(v, bf16x2);
  return __builtin_bit_cast(unsigned, r);
}
DI bf16x8 tr_frag(unsigned a0, unsigned a1) {
  s16x4 lo = __builtin_amdgcn_ds_read_tr16_b64_v4i16((LAS s16x4*)a0);
  s16x4 hi = __builtin_amdgcn_ds_read_tr16_b64_v4i16((LAS s16x4*)a1);
  s16x8 r = __builtin_shufflevector(lo, hi, 0, 1, 2, 3, 4, 5, 6, 7);
  return __builtin_bit_cast(bf16x8, r);
}
DI bf16x8 lds_b128(unsigned a) { return *(LAS const bf16x8*)a; }
DI float swap32_max(float v) {
  auto rr = __builtin_amdgcn_permlane32_swap(__float_as_uint(v), __float_as_uint(v), false, false);
  return fmaxf(__uint_as_float(rr[0]), __uint_as_float(rr[1]));
}
DI float swap32_add(float v) {
  auto rr = __builtin_amdgcn_permlane32_swap(__float_as_uint(v), __float_as_uint(v), false, false);
  return __uint_as_float(rr[0]) + __uint_as_float(rr[1]);
}

__global__ __launch_bounds__(512, 2) void attn_kernel(const char* __restrict__ x, const char* __restrict__ Kc,
                                                      const char* __restrict__ Vc, const char* __restrict__ wq,
                                                      const char* __restrict__ wo, char* __restrict__ slab) {
  __shared__ __attribute__((aligned(1024))) char smem[163840];
  const unsigned lds0 = (unsigned)(size_t)smem;
  const int tid = threadIdx.x, lane = tid & 63;
  const int wid8 = __builtin_amdgcn_readfirstlane(tid >> 6);
  const int wid = wid8 & 3;
  const int qb = wid & 1, role = wid >> 1;
  const int hi = lane >> 5, l31 = lane & 31;
  const int bid = blockIdx.x;
  const int xcd = bid & 7, idx = bid >> 3;
  const int qh = idx & 1, hl = (idx >> 1) & 1, b = idx >> 2, h = xcd * 2 + hl;

  if (wid8 >= 4) {
    const size_t kvo = (size_t)(b * 16 + h) * (512 * 512) + (size_t)((wid >> 1) * 32 + (wid & 1) * 16) * 512;
    const char* Kb = Kc + kvo;
    const char* Vb = Vc + kvo;
    const int rot = b;
    const char* Wqb = wq + (size_t)(wid * 16) * 8192 + h * 512;
    const char* Wob = wo + (size_t)(h * 256 + wid * 8) * 896;
    const char* Xb = x + (size_t)(b * 128 + qh * 64 + wid * 16) * 896;
    const unsigned ldsw = lds0 + wid * 8192;
    const int krot = idx % 7;
    auto issue = [&](auto sc) {
      constexpr int S = decltype(sc)::value;
      constexpr unsigned sl = SCH.slot[S] * SLOT_BYTES;
      const int kt = (S + krot >= 7) ? S + krot - 7 : S + krot;
      if constexpr (S < 7) {
        static_for<0, 2>([&](auto jc) {
          constexpr int j = decltype(jc)::value;
          unsigned voff = (lane >> 3) * 896 + (((lane & 7) ^ (lane >> 3) ^ j) << 4);
          dma16(lds0 + SCR + SCH.slot[S] * 8192 + (2 * wid + j) * 1024, Xb + (size_t)(8 * j) * 896 + 128 * kt, voff);
        });
      }
      static_for<0, 8>([&](auto jc) {
        constexpr int j = decltype(jc)::value;
        if constexpr (S < 7) {
          unsigned voff = hi * 8192 + ((l31 ^ ((2 * (j & 1) + hi) << 2)) << 4);
          dma16(ldsw + sl + j * 1024, Wqb + (size_t)(64 * kt + 2 * j) * 8192, voff);
        } else if constexpr (S < 15) {
          unsigned voff = hi * 512 + ((l31 ^ hi ^ (2 * j)) << 4);
          dma16nt(ldsw + sl + j * 1024, Kb + (size_t)(64 * ((S - 7 + rot) & 7) + 2 * j) * 512, voff);
        } else if constexpr (S < 23) {
          unsigned voff = hi * 512 + ((l31 ^ ((2 * (j & 1) + hi) << 2)) << 4);
          dma16nt(ldsw + sl + j * 1024, Vb + (size_t)(64 * ((S - 15 + rot) & 7) + 2 * j) * 512, voff);
        } else {
          unsigned g = lane ^ ((j & 3) << 2);
          g = (g >= 56) ? g - 8 : g;
          dma16(ldsw + sl + j * 1024, Wob + (size_t)(32 * (S - 23) + j) * 896, g << 4);
        }
      });
    };
    static_for<0, 2>([&](auto ic) { issue(ic); });
    static_for<0, NSTEP>([&](auto sc) {
      constexpr int S = decltype(sc)::value;
      top_sync<SCH.wait[S]>();
      if constexpr (S == 0) { issue(std::integral_constant<int, 2>{}); issue(std::integral_constant<int, 3>{}); }
      if constexpr (SCH.issue_at[S] >= 0) issue(std::integral_constant<int, SCH.issue_at[S]>{});
      if constexpr (S == 6 || S == 14) { asm volatile("s_barrier" ::: "memory"); }
      if constexpr (S == 22) { asm volatile("s_barrier" ::: "memory"); asm volatile("s_barrier" ::: "memory"); }
    });
    return;
  }

  auto top = [&](auto sc) { lds_sync(); };

  const int q4 = (lane & 15) >> 2, p = lane & 3, bb = (lane >> 4) & 1;
  unsigned trc[4];
#pragma unroll
  for (int blk = 0; blk < 4; ++blk) trc[blk] = ((16 * role + 4 * (blk ^ q4) + 2 * bb + (p >> 1)) << 4) + 8 * (p & 1);

  f32x16 acc[4];
#pragma unroll
  for (int i = 0; i < 4; ++i) acc[i] = f32x16{};
  const unsigned xr = lds0 + SCR + (4 * qb + (l31 >> 3)) * 1024 + (l31 & 7) * 128;
  const unsigned xsw = (l31 & 7) ^ ((l31 >> 3) & 1) ^ hi;
  static_for<0, 7>([&](auto tc) {
    constexpr int t = decltype(tc)::value;
    top(tc);
    constexpr unsigned sl = SCH.slot[t] * SLOT_BYTES;
    const unsigned rb = lds0 + sl + (8 * hi + q4) * 512;
    bf16x8 xf[4];
#pragma unroll
    for (int ks = 0; ks < 4; ++ks) xf[ks] = lds_b128(xr + SCH.slot[t] * 8192 + (((2 * ks) ^ xsw) << 4));
    pipe<16, 4>(
        [&](auto ic) { constexpr int i = decltype(ic)::value; constexpr int ks = i >> 2, blk = i & 3;
                       return tr_frag(rb + trc[blk] + (16 * ks) * 512, rb + trc[blk] + (16 * ks + 4) * 512); },
        [&](auto ic, bf16x8 a) { constexpr int i = decltype(ic)::value; constexpr int ks = i >> 2, blk = i & 3;
                       acc[blk] = __builtin_amdgcn_mfma_f32_32x32x16_bf16(a, xf[ks], acc[blk], 0, 0, 0); });
  });
  lds_sync();
  const unsigned qrow = lds0 + SCR + (32 * qb + l31) * 512;
#pragma unroll
  for (int blk = 0; blk < 4; ++blk)
#pragma unroll
    for (int g = 0; g < 4; ++g) {
      u32x2 w = {pk2(acc[blk][4 * g], acc[blk][4 * g + 1]), pk2(acc[blk][4 * g + 2], acc[blk][4 * g + 3])};
      *(LAS u32x2*)(qrow + (((16 * role + 4 * blk + g) ^ (l31 & 15)) << 4) + 8 * hi) = w;
    }

  f32x16 S[8];
  bf16x8 qf[16];
  float mx = -3.0e38f;
  static_for<0, 8>([&](auto tc) {
    constexpr int t = decltype(tc)::value;
    top(std::integral_constant<int, 7 + t>{});
    if constexpr (t == 0) {
#pragma unroll
      for (int s = 0; s < 16; ++s) qf[s] = lds_b128(qrow + (((2 * s + hi) ^ (l31 & 15)) << 4));
    }
    constexpr unsigned sl = SCH.slot[7 + t] * SLOT_BYTES;
    const unsigned kr = lds0 + sl + (32 * role + l31) * 512;
    f32x16 c = f32x16{};
    pipe<16, 4>(
        [&](auto ic) { constexpr int s = decltype(ic)::value; return lds_b128(kr + ((((2 * (s & 7)) + hi) ^ (l31 & 15)) << 4) + (s >> 3) * 256); },
        [&](auto ic, bf16x8 a) { constexpr int s = decltype(ic)::value;
                       c = __builtin_amdgcn_mfma_f32_32x32x16_bf16(a, qf[s], c, 0, 0, 0); });
    S[t] = c;
#pragma unroll
    for (int r = 0; r < 16; ++r) mx = fmaxf(mx, c[r]);
  });
  mx = swap32_max(mx);
  const unsigned xbuf = lds0 + SCR + 16384;
  *(LAS float*)(xbuf + wid * 256 + lane * 4) = mx;
  lds_sync();
  mx = fmaxf(mx, *(LAS const float*)(xbuf + (wid ^ 2) * 256 + lane * 4));

  constexpr float C = 0.0625f * 1.44269504088896340736f;
  const float mC = -mx * C;
  float lsum = 0.f;
  const unsigned pbuf = lds0 + SCR;
  bf16x8 pown[2];
  auto make_p = [&](const f32x16& s, int par) {
    float pe[16];
#pragma unroll
    for (int r = 0; r < 16; ++r) { pe[r] = __builtin_amdgcn_exp2f(fmaf(s[r], C, mC)); lsum += pe[r]; }
#pragma unroll
    for (int sp = 0; sp < 2; ++sp) {
      u32x4 w = {pk2(pe[8 * sp], pe[8 * sp + 1]), pk2(pe[8 * sp + 2], pe[8 * sp + 3]),
                 pk2(pe[8 * sp + 4], pe[8 * sp + 5]), pk2(pe[8 * sp + 6], pe[8 * sp + 7])};
      pown[sp] = __builtin_bit_cast(bf16x8, w);
      *(LAS u32x4*)(pbuf + par * 8192 + wid * 2048 + sp * 1024 + lane * 16) = w;
    }
  };
  make_p(S[0], 0);

  f32x16 o[4];
#pragma unroll
  for (int i = 0; i < 4; ++i) o[i] = f32x16{};
  static_for<0, 8>([&](auto tc) {
    constexpr int t = decltype(tc)::value;
    top(std::integral_constant<int, 15 + t>{});
    constexpr unsigned sl = SCH.slot[15 + t] * SLOT_BYTES;
    bf16x8 pf[2][2];
    pf[0][0] = pown[0]; pf[0][1] = pown[1];
    pf[1][0] = lds_b128(pbuf + (t & 1) * 8192 + (wid ^ 2) * 2048 + lane * 16);
    pf[1][1] = lds_b128(pbuf + (t & 1) * 8192 + (wid ^ 2) * 2048 + 1024 + lane * 16);
    const unsigned vr0 = lds0 + sl + (32 * role + 4 * hi + q4) * 512;
    const unsigned vr1 = lds0 + sl + (32 * (role ^ 1) + 4 * hi + q4) * 512;
    float pe[16];
    unsigned pk[8];
    pipe<16, 4>(
        [&](auto ic) { constexpr int i = decltype(ic)::value; constexpr int u = i >> 3, sp = (i >> 2) & 1, blk = i & 3;
                       const unsigned vr = u ? vr1 : vr0;
                       return tr_frag(vr + trc[blk] + (16 * sp) * 512, vr + trc[blk] + (16 * sp + 8) * 512); },
        [&](auto ic, bf16x8 a) { constexpr int i = decltype(ic)::value; constexpr int u = i >> 3, sp = (i >> 2) & 1, blk = i & 3;
                       o[blk] = __builtin_amdgcn_mfma_f32_32x32x16_bf16(a, pf[u][sp], o[blk], 0, 0, 0);
                       if constexpr (t < 7) {
                         pe[i] = __builtin_amdgcn_exp2f(fmaf(S[t + 1][i], C, mC)); lsum += pe[i];
                         if constexpr (i & 1) pk[i >> 1] = pk2(pe[i - 1], pe[i]);
                         if constexpr ((i & 7) == 7) {
                           constexpr int s4 = (i >> 3) * 4;
                           u32x4 w = {pk[s4], pk[s4 + 1], pk[s4 + 2], pk[s4 + 3]};
                           pown[i >> 3] = __builtin_bit_cast(bf16x8, w);
                           *(LAS u32x4*)(pbuf + ((t + 1) & 1) * 8192 + wid * 2048 + (i >> 3) * 1024 + lane * 16) = w;
                         }
                       } });
    if constexpr (t == 7) {
      float l2 = swap32_add(lsum);
      *(LAS float*)(xbuf + wid * 256 + lane * 4) = l2;
      lsum = l2;
    }
  });

  bf16x8 ofo[4][2], ofp[4][2];
  const unsigned obuf = lds0 + SCR + 0;
  f32x16 acc2[7];
#pragma unroll
  for (int i = 0; i < 7; ++i) acc2[i] = f32x16{};
  lds_sync();
  {
    float lt = lsum + *(LAS const float*)(xbuf + (wid ^ 2) * 256 + lane * 4);
    float inv = 1.0f / lt;
#pragma unroll
    for (int blk = 0; blk < 4; ++blk)
#pragma unroll
      for (int sp = 0; sp < 2; ++sp) {
        u32x4 w = {pk2(o[blk][8 * sp] * inv, o[blk][8 * sp + 1] * inv), pk2(o[blk][8 * sp + 2] * inv, o[blk][8 * sp + 3] * inv),
                   pk2(o[blk][8 * sp + 4] * inv, o[blk][8 * sp + 5] * inv), pk2(o[blk][8 * sp + 6] * inv, o[blk][8 * sp + 7] * inv)};
        ofo[blk][sp] = __builtin_bit_cast(bf16x8, w);
      }
  }
  lds_sync();
#pragma unroll
  for (int blk = 0; blk < 4; ++blk)
#pragma unroll
    for (int sp = 0; sp < 2; ++sp)
      *(LAS u32x4*)(obuf + wid * 8192 + (blk * 2 + sp) * 1024 + lane * 16) = __builtin_bit_cast(u32x4, ofo[blk][sp]);

  unsigned trw[7];
#pragma unroll
  for (int nb = 0; nb < 7; ++nb) trw[nb] = ((4 * ((7 * role + nb) ^ q4) + 2 * bb + (p >> 1)) << 4) + 8 * (p & 1);
  static_for<0, 8>([&](auto tc) {
    constexpr int t = decltype(tc)::value;
    top(std::integral_constant<int, 23 + t>{});
    if constexpr (t == 0) {
#pragma unroll
      for (int blk = 0; blk < 4; ++blk)
#pragma unroll
        for (int sp = 0; sp < 2; ++sp)
          ofp[blk][sp] = lds_b128(obuf + (wid ^ 2) * 8192 + (blk * 2 + sp) * 1024 + lane * 16);
    }
    constexpr unsigned sl = SCH.slot[23 + t] * SLOT_BYTES;
    const unsigned wr = lds0 + sl + (4 * hi + q4) * 1024;
    const bf16x8 bf0 = ((t >> 2) == role) ? ofo[t & 3][0] : ofp[t & 3][0];
    const bf16x8 bf1 = ((t >> 2) == role) ? ofo[t & 3][1] : ofp[t & 3][1];
    pipe<14, 4>(
        [&](auto ic) { constexpr int i = decltype(ic)::value; constexpr int sp = i / 7, nb = i % 7;
                       return tr_frag(wr + trw[nb] + (16 * sp) * 1024, wr + trw[nb] + (16 * sp + 8) * 1024); },
        [&](auto ic, bf16x8 a) { constexpr int i = decltype(ic)::value; constexpr int sp = i / 7, nb = i % 7;
                       acc2[nb] = __builtin_amdgcn_mfma_f32_32x32x16_bf16(a, sp ? bf1 : bf0, acc2[nb], 0, 0, 0); });
  });

  char* sp_ = slab + ((size_t)(((h * 8 + b) * 2 + qh) * 4 + wid) * 14) * 1024 + lane * 16;
#pragma unroll
  for (int nb = 0; nb < 7; ++nb)
#pragma unroll
    for (int g2 = 0; g2 < 2; ++g2) {
      u32x4 w = {pk2(acc2[nb][8 * g2], acc2[nb][8 * g2 + 1]), pk2(acc2[nb][8 * g2 + 2], acc2[nb][8 * g2 + 3]),
                 pk2(acc2[nb][8 * g2 + 4], acc2[nb][8 * g2 + 5]), pk2(acc2[nb][8 * g2 + 6], acc2[nb][8 * g2 + 7])};
      __builtin_nontemporal_store(w, (u32x4*)(sp_ + (nb * 2 + g2) * 1024));
    }
}

__global__ __launch_bounds__(256) void reduce_kernel(const u32x4* __restrict__ slab, char* __restrict__ out) {
  const int i = blockIdx.x * 256 + threadIdx.x;
  float s[8];
#pragma unroll
  for (int k = 0; k < 8; ++k) s[k] = 0.f;
  u32x4 v[16];
#pragma unroll
  for (int h = 0; h < 16; ++h) v[h] = __builtin_nontemporal_load(&slab[(size_t)h * 57344 + i]);
#pragma unroll
  for (int h = 0; h < 16; ++h)
#pragma unroll
    for (int k = 0; k < 4; ++k) {
      s[2 * k] += __uint_as_float(v[h][k] << 16);
      s[2 * k + 1] += __uint_as_float(v[h][k] & 0xffff0000u);
    }
  const int lane = i & 63, g2 = (i >> 6) & 1;
  int rest = i >> 7;
  const int nb = rest % 7; rest /= 7;
  const int wid = rest & 3, qh = (rest >> 2) & 1, b = rest >> 3;
  const int row = b * 128 + qh * 64 + (wid & 1) * 32 + (lane & 31);
  const int n0 = 32 * (7 * (wid >> 1) + nb) + 16 * g2 + 4 * (lane >> 5);
  char* o = out + ((size_t)row * 448 + n0) * 2;
  u32x2 w0 = {pk2(s[0], s[1]), pk2(s[2], s[3])};
  u32x2 w1 = {pk2(s[4], s[5]), pk2(s[6], s[7])};
  *(u32x2*)o = w0;
  *(u32x2*)(o + 16) = w1;
}

extern "C" void kernel_launch(void* const* d_in, const int* in_sizes, int n_in, void* d_out, int out_size,
                              void* d_ws, size_t ws_size, hipStream_t stream) {
  const char* x = (const char*)d_in[0];
  const char* k = (const char*)d_in[1];
  const char* v = (const char*)d_in[2];
  const char* wq = (const char*)d_in[3];
  const char* wo = (const char*)d_in[4];
  char* slab = (char*)d_ws;
  attn_kernel<<<256, 512, 0, stream>>>(x, k, v, wq, wo, slab);
  reduce_kernel<<<224, 256, 0, stream>>>((const u32x4*)slab, (char*)d_out);
}
```

```cpp
#include <hip/hip_runtime.h>
#include <hip/hip_bf16.h>
#include <stdint.h>
#include <type_traits>

typedef __bf16 bf16x8 __attribute__((ext_vector_type(8)));
typedef __bf16 bf16x2 __attribute__((ext_vector_type(2)));
typedef short s16x4 __attribute__((ext_vector_type(4)));
typedef short s16x8 __attribute__((ext_vector_type(8)));
typedef float f32x2 __attribute__((ext_vector_type(2)));
typedef float f32x16 __attribute__((ext_vector_type(16)));
typedef unsigned u32x2 __attribute__((ext_vector_type(2)));
typedef unsigned u32x4 __attribute__((ext_vector_type(4)));
#define LAS __attribute__((address_space(3)))
#define DI __device__ __forceinline__

constexpr int NSTEP = 30;
constexpr int NSLOT = 4;
constexpr unsigned SLOT_BYTES = 32768;
constexpr unsigned SCR = NSLOT * SLOT_BYTES;

struct Sched { int slot[NSTEP]; int wait[NSTEP]; };
constexpr Sched make_sched() {
  Sched s{};
  int cum = 0;
  int endT[NSTEP] = {}; int endX[8] = {};
  auto X = [&](int j) { cum += 2; endX[j] = cum; };
  auto T = [&](int t) { cum += 8; endT[t] = cum; };
  X(0); T(0); X(1); T(1);
  for (int t = 0; t < NSTEP; ++t) {
    s.slot[t] = t % NSLOT;
    int need = endT[t];
    if (t + 1 <= 6 && endX[t + 1] > need) need = endX[t + 1];
    s.wait[t] = cum - need;
    if (t == 0) { X(2); T(2); X(3); T(3); }
    else if (t + 3 < NSTEP) { if (t + 3 <= 6) X(t + 3); T(t + 3); }
  }
  return s;
}
constexpr Sched SCH = make_sched();

template <int B, int E, class F> DI void static_for(F&& f) {
  if constexpr (B < E) { f(std::integral_constant<int, B>{}); static_for<B + 1, E>(f); }
}

template <int N, int D, class L, class M> DI void pipe(L&& load, M&& mma) {
  bf16x8 fr[N];
  static_for<0, D>([&](auto ic) { fr[decltype(ic)::value] = load(ic); });
  __builtin_amdgcn_sched_barrier(0);
  static_for<0, N>([&](auto ic) {
    constexpr int i = decltype(ic)::value;
    if constexpr (i + D < N) fr[i + D] = load(std::integral_constant<int, i + D>{});
    mma(ic, fr[i]);
    __builtin_amdgcn_sched_barrier(0);
  });
}

DI void dma16(unsigned lds_addr, const char* sbase, unsigned voff) {
  asm volatile("s_mov_b32 m0, %0\n\ts_nop 0\n\tglobal_load_lds_dwordx4 %1, %2"
               :: "s"(lds_addr), "v"(voff), "s"(sbase) : "memory");
}
DI void dma16nt(unsigned lds_addr, const char* sbase, unsigned voff) {
  asm volatile("s_mov_b32 m0, %0\n\ts_nop 0\n\tglobal_load_lds_dwordx4 %1, %2 nt"
               :: "s"(lds_addr), "v"(voff), "s"(sbase) : "memory");
}
template <int N> DI void top_sync() {
  asm volatile("s_waitcnt vmcnt(%0) lgkmcnt(0)\n\ts_barrier" :: "n"(N) : "memory");
}
DI void lds_sync() { asm volatile("s_waitcnt lgkmcnt(0)\n\ts_barrier" ::: "memory"); }

DI unsigned pk2(float a, float b) {
  f32x2 v = {a, b};
  bf16x2 r = __builtin_convertvector(v, bf16x2);
  return __builtin_bit_cast(unsigned, r);
}
DI bf16x8 tr_frag(unsigned a0, unsigned a1) {
  s16x4 lo = __builtin_amdgcn_ds_read_tr16_b64_v4i16((LAS s16x4*)a0);
  s16x4 hi = __builtin_amdgcn_ds_read_tr16_b64_v4i16((LAS s16x4*)a1);
  s16x8 r = __builtin_shufflevector(lo, hi, 0, 1, 2, 3, 4, 5, 6, 7);
  return __builtin_bit_cast(bf16x8, r);
}
DI bf16x8 lds_b128(unsigned a) { return *(LAS const bf16x8*)a; }
DI float swap32_max(float v) {
  auto rr = __builtin_amdgcn_permlane32_swap(__float_as_uint(v), __float_as_uint(v), false, false);
  return fmaxf(__uint_as_float(rr[0]), __uint_as_float(rr[1]));
}
DI float swap32_add(float v) {
  auto rr = __builtin_amdgcn_permlane32_swap(__float_as_uint(v), __float_as_uint(v), false, false);
  return __uint_as_float(rr[0]) + __uint_as_float(rr[1]);
}

__global__ __launch_bounds__(512, 2) void attn_kernel(const char* __restrict__ x, const char* __restrict__ Kc,
                                                      const char* __restrict__ Vc, const char* __restrict__ wq,
                                                      const char* __restrict__ wo, char* __restrict__ slab) {
  __shared__ __attribute__((aligned(1024))) char smem[163840];
  const unsigned lds0 = (unsigned)(size_t)smem;
  const int tid = threadIdx.x, lane = tid & 63;
  const int wid8 = __builtin_amdgcn_readfirstlane(tid >> 6);
  const int wid = wid8 & 3;
  const int qb = wid & 1, role = wid >> 1;
  const int hi = lane >> 5, l31 = lane & 31;
  const int bid = blockIdx.x;
  const int xcd = bid & 7, idx = bid >> 3;
  const int qh = idx & 1, hl = (idx >> 1) & 1, b = idx >> 2, h = xcd * 2 + hl;

  if (wid8 >= 4) {
    const size_t kvo = (size_t)(b * 16 + h) * (512 * 512) + (size_t)((wid >> 1) * 32 + (wid & 1) * 16) * 512;
    const char* Kb = Kc + kvo;
    const char* Vb = Vc + kvo;
    const int rot = b;
    const char* Wqb = wq + (size_t)(wid * 16) * 8192 + h * 512;
    const char* Wob = wo + (size_t)(h * 256 + wid * 64) * 896;
    const char* Xb = x + (size_t)(b * 128 + qh * 64 + wid * 16) * 896;
    const unsigned ldsw = lds0 + wid * 8192;
    const int krot = idx % 7;
    auto issue_x = [&](auto sc) {
      constexpr int S = decltype(sc)::value;
      const int kt = (S + krot >= 7) ? S + krot - 7 : S + krot;
      static_for<0, 2>([&](auto jc) {
        constexpr int j = decltype(jc)::value;
        unsigned voff = (lane >> 3) * 896 + (((lane & 7) ^ (lane >> 3) ^ j) << 4);
        dma16(lds0 + SCR + (S % NSLOT) * 8192 + (2 * wid + j) * 1024, Xb + (size_t)(8 * j) * 896 + 128 * kt, voff);
      });
    };
    auto issue = [&](auto sc) {
      constexpr int S = decltype(sc)::value;
      constexpr unsigned sl = SCH.slot[S] * SLOT_BYTES;
      const int kt = (S + krot >= 7) ? S + krot - 7 : S + krot;
      static_for<0, 8>([&](auto jc) {
        constexpr int j = decltype(jc)::value;
        if constexpr (S < 7) {
          unsigned voff = hi * 8192 + ((l31 ^ ((2 * (j & 1) + hi) << 2)) << 4);
          dma16(ldsw + sl + j * 1024, Wqb + (size_t)(64 * kt + 2 * j) * 8192, voff);
        } else if constexpr (S < 15) {
          unsigned voff = hi * 512 + ((l31 ^ hi ^ (2 * j)) << 4);
          dma16nt(ldsw + sl + j * 1024, Kb + (size_t)(64 * ((S - 7 + rot) & 7) + 2 * j) * 512, voff);
        } else if constexpr (S < 23) {
          unsigned voff = hi * 512 + ((l31 ^ ((2 * (j & 1) + hi) << 2)) << 4);
          dma16nt(ldsw + sl + j * 1024, Vb + (size_t)(64 * ((S - 15 + rot) & 7) + 2 * j) * 512, voff);
        } else {
          unsigned voff = (lane >> 3) * 896 + (((lane & 7) ^ (((lane >> 4) & 1) << 2)) << 4);
          const int ot = (S - 23 + krot >= 7) ? S - 23 + krot - 7 : S - 23 + krot;
          dma16(ldsw + sl + j * 1024, Wob + (size_t)(8 * j) * 896 + 128 * ot, voff);
        }
      });
    };
#define IC(v) std::integral_constant<int, (v)>{}
    issue_x(IC(0)); issue(IC(0)); issue_x(IC(1)); issue(IC(1));
    static_for<0, NSTEP>([&](auto sc) {
      constexpr int S = decltype(sc)::value;
      top_sync<SCH.wait[S]>();
      if constexpr (S == 0) { issue_x(IC(2)); issue(IC(2)); issue_x(IC(3)); issue(IC(3)); }
      else if constexpr (S + 3 < NSTEP) {
        if constexpr (S + 3 <= 6) issue_x(IC(S + 3));
        issue(IC(S + 3));
      }
      if constexpr (S == 14 || S == 22) { asm volatile("s_barrier" ::: "memory"); }
    });
    return;
  }

  auto top = [&](auto sc) { lds_sync(); };

  const char* Kt = Kc + (size_t)(b * 16 + h) * (512 * 512) + qh * 16384 + wid * 4096;
  unsigned td0 = 0, td1 = 0, td2 = 0, td3 = 0, td4 = 0;

  const int q4 = (lane & 15) >> 2, p = lane & 3, bb = (lane >> 4) & 1;
  unsigned trc[4];
#pragma unroll
  for (int blk = 0; blk < 4; ++blk) trc[blk] = ((16 * role + 4 * (blk ^ q4) + 2 * bb + (p >> 1)) << 4) + 8 * (p & 1);

  unsigned trq[4];
#pragma unroll
  for (int blk = 0; blk < 4; ++blk) trq[blk] = ((16 * role + 4 * (blk ^ q4) + 2 * bb + (p & 1)) << 4) + 8 * (p >> 1);
  f32x16 acc[4];
#pragma unroll
  for (int i = 0; i < 4; ++i) acc[i] = f32x16{};
  const unsigned xr = lds0 + SCR + (4 * qb + (l31 >> 3)) * 1024 + (l31 & 7) * 128;
  const unsigned xsw = (l31 & 7) ^ ((l31 >> 3) & 1) ^ hi;
  bf16x8 xf[2][4];
  static_for<0, 7>([&](auto tc) {
    constexpr int t = decltype(tc)::value;
    top(tc);
    constexpr unsigned sl = SCH.slot[t] * SLOT_BYTES;
    const unsigned rb = lds0 + sl + (8 * hi + q4) * 512;
    if constexpr (t == 0) {
#pragma unroll
      for (int ks = 0; ks < 4; ++ks) xf[0][ks] = lds_b128(xr + (((2 * ks) ^ xsw) << 4));
    }
    if constexpr (t == 1) asm volatile("global_load_dword %0, %1, %2" : "=v"(td0) : "v"((unsigned)(lane * 64)), "s"(Kt + (((6 + b) & 7) << 15)));
    if constexpr (t == 2) asm volatile("global_load_dword %0, %1, %2" : "=v"(td1) : "v"((unsigned)(lane * 64)), "s"(Kt + (((7 + b) & 7) << 15)));
    pipe<16, 4>(
        [&](auto ic) { constexpr int i = decltype(ic)::value; constexpr int ks = i >> 2, blk = i & 3;
                       return tr_frag(rb + trq[blk] + (16 * ks) * 512, rb + trq[blk] + (16 * ks + 4) * 512); },
        [&](auto ic, bf16x8 a) { constexpr int i = decltype(ic)::value; constexpr int ks = i >> 2, blk = i & 3;
                       acc[blk] = __builtin_amdgcn_mfma_f32_32x32x16_bf16(a, xf[t & 1][ks], acc[blk], 0, 0, 0);
                       if constexpr (t < 6 && i >= 4 && i < 8)
                         xf[(t + 1) & 1][i - 4] = lds_b128(xr + ((t + 1) % NSLOT) * 8192 + (((2 * (i - 4)) ^ xsw) << 4)); });
  });
  asm volatile("s_waitcnt vmcnt(0)" :: "v"(td0), "v"(td1), "v"(td2), "v"(td3), "v"(td4));
  bf16x8 qf[16];
#pragma unroll
  for (int blk = 0; blk < 4; ++blk)
#pragma unroll
    for (int sp = 0; sp < 2; ++sp) {
      u32x4 w = {pk2(acc[blk][8 * sp], acc[blk][8 * sp + 1]), pk2(acc[blk][8 * sp + 2], acc[blk][8 * sp + 3]),
                 pk2(acc[blk][8 * sp + 4], acc[blk][8 * sp + 5]), pk2(acc[blk][8 * sp + 6], acc[blk][8 * sp + 7])};
      qf[2 * blk + sp] = __builtin_bit_cast(bf16x8, w);
      *(LAS u32x4*)(lds0 + SCR + wid * 8192 + (2 * blk + sp) * 1024 + lane * 16) = w;
    }

  f32x16 S[8];
  float mx = -3.0e38f;
  static_for<0, 8>([&](auto tc) {
    constexpr int t = decltype(tc)::value;
    top(std::integral_constant<int, 7 + t>{});
    if constexpr (t == 0) {
#pragma unroll
      for (int s = 0; s < 8; ++s) qf[8 + s] = lds_b128(lds0 + SCR + (wid ^ 2) * 8192 + s * 1024 + lane * 16);
    }
    constexpr unsigned sl = SCH.slot[7 + t] * SLOT_BYTES;
    const unsigned kr = lds0 + sl + (32 * role + l31) * 512;
    const unsigned kro = kr + role * 256, krp = kr + (role ^ 1) * 256;
    f32x16 c = f32x16{};
    pipe<16, 4>(
        [&](auto ic) { constexpr int s = decltype(ic)::value; return lds_b128((s >> 3 ? krp : kro) + ((((2 * (s & 7)) + hi) ^ (l31 & 15)) << 4)); },
        [&](auto ic, bf16x8 a) { constexpr int s = decltype(ic)::value;
                       c = __builtin_amdgcn_mfma_f32_32x32x16_bf16(a, qf[s], c, 0, 0, 0); });
    S[t] = c;
#pragma unroll
    for (int r = 0; r < 16; ++r) mx = fmaxf(mx, c[r]);
  });
  mx = swap32_max(mx);
  const unsigned xbuf = lds0 + SCR + 16384;
  *(LAS float*)(xbuf + wid * 256 + lane * 4) = mx;
  lds_sync();
  mx = fmaxf(mx, *(LAS const float*)(xbuf + (wid ^ 2) * 256 + lane * 4));

  constexpr float C = 0.0625f * 1.44269504088896340736f;
  const float mC = -mx * C;
  float lsum = 0.f;
  const unsigned pbuf = lds0 + SCR;
  bf16x8 pown[2];
  auto make_p = [&](const f32x16& s, int par) {
    float pe[16];
#pragma unroll
    for (int r = 0; r < 16; ++r) { pe[r] = __builtin_amdgcn_exp2f(fmaf(s[r], C, mC)); lsum += pe[r]; }
#pragma unroll
    for (int sp = 0; sp < 2; ++sp) {
      u32x4 w = {pk2(pe[8 * sp], pe[8 * sp + 1]), pk2(pe[8 * sp + 2], pe[8 * sp + 3]),
                 pk2(pe[8 * sp + 4], pe[8 * sp + 5]), pk2(pe[8 * sp + 6], pe[8 * sp + 7])};
      pown[sp] = __builtin_bit_cast(bf16x8, w);
      *(LAS u32x4*)(pbuf + par * 8192 + wid * 2048 + sp * 1024 + lane * 16) = w;
    }
  };
  make_p(S[0], 0);

  f32x16 o[4];
#pragma unroll
  for (int i = 0; i < 4; ++i) o[i] = f32x16{};
  static_for<0, 8>([&](auto tc) {
    constexpr int t = decltype(tc)::value;
    top(std::integral_constant<int, 15 + t>{});
    constexpr unsigned sl = SCH.slot[15 + t] * SLOT_BYTES;
    bf16x8 pf[2][2];
    pf[0][0] = pown[0]; pf[0][1] = pown[1];
    pf[1][0] = lds_b128(pbuf + (t & 1) * 8192 + (wid ^ 2) * 2048 + lane * 16);
    pf[1][1] = lds_b128(pbuf + (t & 1) * 8192 + (wid ^ 2) * 2048 + 1024 + lane * 16);
    if constexpr (t == 7) lsum += *(LAS const float*)(xbuf + (wid ^ 2) * 256 + lane * 4);
    const unsigned vr0 = lds0 + sl + (32 * role + 4 * hi + q4) * 512;
    const unsigned vr1 = lds0 + sl + (32 * (role ^ 1) + 4 * hi + q4) * 512;
    float pe[16];
    unsigned pk[8];
    pipe<16, 4>(
        [&](auto ic) { constexpr int i = decltype(ic)::value; constexpr int u = i >> 3, sp = (i >> 2) & 1, blk = i & 3;
                       const unsigned vr = u ? vr1 : vr0;
                       return tr_frag(vr + trc[blk] + (16 * sp) * 512, vr + trc[blk] + (16 * sp + 8) * 512); },
        [&](auto ic, bf16x8 a) { constexpr int i = decltype(ic)::value; constexpr int u = i >> 3, sp = (i >> 2) & 1, blk = i & 3;
                       o[blk] = __builtin_amdgcn_mfma_f32_32x32x16_bf16(a, pf[u][sp], o[blk], 0, 0, 0);
                       if constexpr (t < 7) {
                         pe[i] = __builtin_amdgcn_exp2f(fmaf(S[t + 1][i], C, mC)); lsum += pe[i];
                         if constexpr (i & 1) pk[i >> 1] = pk2(pe[i - 1], pe[i]);
                         if constexpr ((i & 7) == 7) {
                           constexpr int s4 = (i >> 3) * 4;
                           u32x4 w = {pk[s4], pk[s4 + 1], pk[s4 + 2], pk[s4 + 3]};
                           pown[i >> 3] = __builtin_bit_cast(bf16x8, w);
                           *(LAS u32x4*)(pbuf + ((t + 1) & 1) * 8192 + wid * 2048 + (i >> 3) * 1024 + lane * 16) = w;
                         }
                       } });
    if constexpr (t == 6) {
      float l2 = swap32_add(lsum);
      *(LAS float*)(xbuf + wid * 256 + lane * 4) = l2;
      lsum = l2;
    }
  });

  bf16x8 of[16];
#pragma unroll
  for (int blk = 0; blk < 4; ++blk)
#pragma unroll
    for (int sp = 0; sp < 2; ++sp) {
      u32x4 w = {pk2(o[blk][8 * sp], o[blk][8 * sp + 1]), pk2(o[blk][8 * sp + 2], o[blk][8 * sp + 3]),
                 pk2(o[blk][8 * sp + 4], o[blk][8 * sp + 5]), pk2(o[blk][8 * sp + 6], o[blk][8 * sp + 7])};
      of[blk * 2 + sp] = __builtin_bit_cast(bf16x8, w);
    }
  const float inv = 1.0f / lsum;
  lds_sync();
#pragma unroll
  for (int s = 0; s < 8; ++s) *(LAS u32x4*)(lds0 + SCR + wid * 8192 + s * 1024 + lane * 16) = __builtin_bit_cast(u32x4, of[s]);

  int ln = lane;
  asm volatile("" : "+v"(ln));
  const unsigned tro = (((4 * role + 2 * ((ln >> 4) & 1) + ((ln & 3) >> 1)) ^ ((((ln & 15) >> 2) >> 1) << 2)) << 4) + 8 * (ln & 1) + (4 * (ln >> 5) + ((ln & 15) >> 2)) * 128;
  char* sp_ = slab + ((size_t)(((h * 8 + b) * 2 + qh) * 4 + wid) * 14) * 1024 + ln * 16;
  f32x16 acc2[2];
  const int orot = idx % 7;
  auto store_half = [&](const f32x16& a, int step, int g2) {
    u32x4 w = {pk2(a[8 * g2] * inv, a[8 * g2 + 1] * inv), pk2(a[8 * g2 + 2] * inv, a[8 * g2 + 3] * inv),
               pk2(a[8 * g2 + 4] * inv, a[8 * g2 + 5] * inv), pk2(a[8 * g2 + 6] * inv, a[8 * g2 + 7] * inv)};
    const int ot = (step + orot >= 7) ? step + orot - 7 : step + orot;
    __builtin_nontemporal_store(w, (u32x4*)(sp_ + (ot * 2 + g2) * 1024));
  };
  static_for<0, 7>([&](auto tc) {
    constexpr int t = decltype(tc)::value;
    top(std::integral_constant<int, 23 + t>{});
    if constexpr (t == 0) {
#pragma unroll
      for (int s = 0; s < 8; ++s) of[8 + s] = lds_b128(lds0 + SCR + (wid ^ 2) * 8192 + s * 1024 + lane * 16);
    }
    constexpr unsigned sl = SCH.slot[23 + t] * SLOT_BYTES;
    const unsigned wro = lds0 + sl + tro + role * 16384, wrp = lds0 + sl + tro + (role ^ 1) * 16384;
    f32x16 c = f32x16{};
    pipe<16, 4>(
        [&](auto ic) { constexpr int i = decltype(ic)::value; const unsigned wr = (i >> 3) ? wrp : wro;
                       return tr_frag(wr + (i & 7) * 2048, wr + (i & 7) * 2048 + 1024); },
        [&](auto ic, bf16x8 a) { constexpr int i = decltype(ic)::value;
                       c = __builtin_amdgcn_mfma_f32_32x32x16_bf16(a, of[i], c, 0, 0, 0);
                       if constexpr (t > 0 && i == 2) store_half(acc2[(t - 1) & 1], t - 1, 0);
                       if constexpr (t > 0 && i == 6) store_half(acc2[(t - 1) & 1], t - 1, 1); });
    acc2[t & 1] = c;
  });
  store_half(acc2[0], 6, 0);
  store_half(acc2[0], 6, 1);
}

__global__ __launch_bounds__(256) void reduce_kernel(const u32x4* __restrict__ slab, char* __restrict__ out) {
  const int i = blockIdx.x * 256 + threadIdx.x;
  float s[8];
#pragma unroll
  for (int k = 0; k < 8; ++k) s[k] = 0.f;
  u32x4 v[16];
#pragma unroll
  for (int h = 0; h < 16; ++h) v[h] = __builtin_nontemporal_load(&slab[(size_t)h * 57344 + i]);
#pragma unroll
  for (int h = 0; h < 16; ++h)
#pragma unroll
    for (int k = 0; k < 4; ++k) {
      s[2 * k] += __uint_as_float(v[h][k] << 16);
      s[2 * k + 1] += __uint_as_float(v[h][k] & 0xffff0000u);
    }
  const int lane = i & 63, g2 = (i >> 6) & 1;
  int rest = i >> 7;
  const int nb = rest % 7; rest /= 7;
  const int wid = rest & 3, qh = (rest >> 2) & 1, b = rest >> 3;
  const int row = b * 128 + qh * 64 + (wid & 1) * 32 + (lane & 31);
  const int n0 = 32 * (2 * nb + (wid >> 1)) + 16 * g2 + 4 * (lane >> 5);
  char* o = out + ((size_t)row * 448 + n0) * 2;
  u32x2 w0 = {pk2(s[0], s[1]), pk2(s[2], s[3])};
  u32x2 w1 = {pk2(s[4], s[5]), pk2(s[6], s[7])};
  *(u32x2*)o = w0;
  *(u32x2*)(o + 16) = w1;
}

extern "C" void kernel_launch(void* const* d_in, const int* in_sizes, int n_in, void* d_out, int out_size,
                              void* d_ws, size_t ws_size, hipStream_t stream) {
  const char* x = (const char*)d_in[0];
  const char* k = (const char*)d_in[1];
  const char* v = (const char*)d_in[2];
  const char* wq = (const char*)d_in[3];
  const char* wo = (const char*)d_in[4];
  char* slab = (char*)d_ws;
  attn_kernel<<<256, 512, 0, stream>>>(x, k, v, wq, wo, slab);
  reduce_kernel<<<224, 256, 0, stream>>>((const u32x4*)slab, (char*)d_out);
}
```

```cpp
#include <hip/hip_runtime.h>
#include <hip/hip_bf16.h>
#include <stdint.h>
#include <type_traits>

typedef __bf16 bf16x8 __attribute__((ext_vector_type(8)));
typedef __bf16 bf16x2 __attribute__((ext_vector_type(2)));
typedef short s16x4 __attribute__((ext_vector_type(4)));
typedef short s16x8 __attribute__((ext_vector_type(8)));
typedef float f32x2 __attribute__((ext_vector_type(2)));
typedef float f32x4 __attribute__((ext_vector_type(4)));
typedef float f32x16 __attribute__((ext_vector_type(16)));
typedef unsigned u32x2 __attribute__((ext_vector_type(2)));
typedef unsigned u32x4 __attribute__((ext_vector_type(4)));
#define LAS __attribute__((address_space(3)))
#define DI __device__ __forceinline__

constexpr int NSTEP = 30;
constexpr int NSLOT = 4;
constexpr unsigned SLOT_BYTES = 32768;
constexpr unsigned SCR = NSLOT * SLOT_BYTES;

struct Sched { int slot[NSTEP]; int wait[NSTEP]; };
constexpr Sched make_sched() {
  Sched s{};
  int cum = 0;
  int endT[NSTEP] = {}; int endX[8] = {};
  auto X = [&](int j) { cum += 2; endX[j] = cum; };
  auto T = [&](int t) { cum += 8; endT[t] = cum; };
  X(0); T(0); X(1); T(1);
  for (int t = 0; t < NSTEP; ++t) {
    s.slot[t] = t % NSLOT;
    int need = endT[t];
    if (t + 1 <= 6 && endX[t + 1] > need) need = endX[t + 1];
    s.wait[t] = cum - need;
    if (t == 0) { X(2); T(2); X(3); T(3); }
    else if (t + 3 < NSTEP) { if (t + 3 <= 6) X(t + 3); T(t + 3); }
  }
  return s;
}
constexpr Sched SCH = make_sched();

template <int B, int E, class F> DI void static_for(F&& f) {
  if constexpr (B < E) { f(std::integral_constant<int, B>{}); static_for<B + 1, E>(f); }
}

template <int N, int D, class L, class M> DI void pipe(L&& load, M&& mma) {
  bf16x8 fr[N];
  static_for<0, D>([&](auto ic) { fr[decltype(ic)::value] = load(ic); });
  __builtin_amdgcn_sched_barrier(0);
  static_for<0, N>([&](auto ic) {
    constexpr int i = decltype(ic)::value;
    if constexpr (i + D < N) fr[i + D] = load(std::integral_constant<int, i + D>{});
    mma(ic, fr[i]);
    __builtin_amdgcn_sched_barrier(0);
  });
}

DI void dma16(unsigned lds_addr, const char* sbase, unsigned voff) {
  asm volatile("s_mov_b32 m0, %0\n\ts_nop 0\n\tglobal_load_lds_dwordx4 %1, %2"
               :: "s"(lds_addr), "v"(voff), "s"(sbase) : "memory");
}
DI void dma16nt(unsigned lds_addr, const char* sbase, unsigned voff) {
  asm volatile("s_mov_b32 m0, %0\n\ts_nop 0\n\tglobal_load_lds_dwordx4 %1, %2 nt"
               :: "s"(lds_addr), "v"(voff), "s"(sbase) : "memory");
}
template <int N> DI void top_sync() {
  asm volatile("s_waitcnt vmcnt(%0) lgkmcnt(0)\n\ts_barrier" :: "n"(N) : "memory");
}
DI void lds_sync() { asm volatile("s_waitcnt lgkmcnt(0)\n\ts_barrier" ::: "memory"); }

DI unsigned pk2(float a, float b) {
  f32x2 v = {a, b};
  bf16x2 r = __builtin_convertvector(v, bf16x2);
  return __builtin_bit_cast(unsigned, r);
}
DI bf16x8 tr_frag(unsigned a0, unsigned a1) {
  s16x4 lo = __builtin_amdgcn_ds_read_tr16_b64_v4i16((LAS s16x4*)a0);
  s16x4 hi = __builtin_amdgcn_ds_read_tr16_b64_v4i16((LAS s16x4*)a1);
  s16x8 r = __builtin_shufflevector(lo, hi, 0, 1, 2, 3, 4, 5, 6, 7);
  return __builtin_bit_cast(bf16x8, r);
}
DI bf16x8 lds_b128(unsigned a) { return *(LAS const bf16x8*)a; }
DI float swap32_max(float v) {
  auto rr = __builtin_amdgcn_permlane32_swap(__float_as_uint(v), __float_as_uint(v), false, false);
  return fmaxf(__uint_as_float(rr[0]), __uint_as_float(rr[1]));
}
DI float swap32_add(float v) {
  auto rr = __builtin_amdgcn_permlane32_swap(__float_as_uint(v), __float_as_uint(v), false, false);
  return __uint_as_float(rr[0]) + __uint_as_float(rr[1]);
}

__global__ __launch_bounds__(512, 2) void attn_kernel(const char* __restrict__ x, const char* __restrict__ Kc,
                                                      const char* __restrict__ Vc, const char* __restrict__ wq,
                                                      const char* __restrict__ wo, char* __restrict__ slab) {
  __shared__ __attribute__((aligned(1024))) char smem[163840];
  const unsigned lds0 = (unsigned)(size_t)smem;
  const int tid = threadIdx.x, lane = tid & 63;
  const int wid8 = __builtin_amdgcn_readfirstlane(tid >> 6);
  const int wid = wid8 & 3;
  const int qb = wid & 1, role = wid >> 1;
  const int hi = lane >> 5, l31 = lane & 31;
  const int bid = blockIdx.x;
  const int xcd = bid & 7, idx = bid >> 3;
  const int qh = idx & 1, hl = (idx >> 1) & 1, b = idx >> 2, h = xcd * 2 + hl;

  if (wid8 >= 4) {
    const size_t kvo = (size_t)(b * 16 + h) * (512 * 512) + (size_t)((wid >> 1) * 32 + (wid & 1) * 16) * 512;
    const char* Kb = Kc + kvo;
    const char* Vb = Vc + kvo;
    const int rot = b;
    const char* Wqb = wq + (size_t)(wid * 16) * 8192 + h * 512;
    const char* Wob = wo + (size_t)(h * 256 + wid * 64) * 896;
    const char* Xb = x + (size_t)(b * 128 + qh * 64 + wid * 16) * 896;
    const unsigned ldsw = lds0 + wid * 8192;
    const int krot = idx % 7;
    auto issue_x = [&](auto sc) {
      constexpr int S = decltype(sc)::value;
      const int kt = (S + krot >= 7) ? S + krot - 7 : S + krot;
      static_for<0, 2>([&](auto jc) {
        constexpr int j = decltype(jc)::value;
        unsigned voff = (lane >> 3) * 896 + (((lane & 7) ^ (lane >> 3) ^ j) << 4);
        dma16(lds0 + SCR + (S % NSLOT) * 8192 + (2 * wid + j) * 1024, Xb + (size_t)(8 * j) * 896 + 128 * kt, voff);
      });
    };
    auto issue = [&](auto sc) {
      constexpr int S = decltype(sc)::value;
      constexpr unsigned sl = SCH.slot[S] * SLOT_BYTES;
      const int kt = (S + krot >= 7) ? S + krot - 7 : S + krot;
      static_for<0, 8>([&](auto jc) {
        constexpr int j = decltype(jc)::value;
        if constexpr (S < 7) {
          unsigned voff = hi * 8192 + ((l31 ^ ((2 * (j & 1) + hi) << 2)) << 4);
          dma16(ldsw + sl + j * 1024, Wqb + (size_t)(64 * kt + 2 * j) * 8192, voff);
        } else if constexpr (S < 15) {
          unsigned voff = hi * 512 + ((l31 ^ hi ^ (2 * j)) << 4);
          dma16nt(ldsw + sl + j * 1024, Kb + (size_t)(64 * ((S - 7 + rot) & 7) + 2 * j) * 512, voff);
        } else if constexpr (S < 23) {
          unsigned voff = hi * 512 + ((l31 ^ ((2 * (j & 1) + hi) << 2)) << 4);
          dma16nt(ldsw + sl + j * 1024, Vb + (size_t)(64 * ((S - 15 + rot) & 7) + 2 * j) * 512, voff);
        } else {
          unsigned voff = (lane >> 3) * 896 + (((lane & 7) ^ (((lane >> 4) & 1) << 2)) << 4);
          const int ot = (S - 23 + krot >= 7) ? S - 23 + krot - 7 : S - 23 + krot;
          dma16(ldsw + sl + j * 1024, Wob + (size_t)(8 * j) * 896 + 128 * ot, voff);
        }
      });
    };
#define IC(v) std::integral_constant<int, (v)>{}
    issue_x(IC(0)); issue(IC(0)); issue_x(IC(1)); issue(IC(1));
    static_for<0, NSTEP>([&](auto sc) {
      constexpr int S = decltype(sc)::value;
      top_sync<SCH.wait[S]>();
      if constexpr (S == 0) { issue_x(IC(2)); issue(IC(2)); issue_x(IC(3)); issue(IC(3)); }
      else if constexpr (S + 3 < NSTEP) {
        if constexpr (S + 3 <= 6) issue_x(IC(S + 3));
        issue(IC(S + 3));
      }
      if constexpr (S == 14 || S == 22) { asm volatile("s_barrier" ::: "memory"); }
    });
    return;
  }

  auto top = [&](auto sc) { lds_sync(); };

  const int q4 = (lane & 15) >> 2, p = lane & 3, bb = (lane >> 4) & 1;
  unsigned trc[4];
#pragma unroll
  for (int blk = 0; blk < 4; ++blk) trc[blk] = ((16 * role + 4 * (blk ^ q4) + 2 * bb + (p >> 1)) << 4) + 8 * (p & 1);

  unsigned trq[4];
#pragma unroll
  for (int blk = 0; blk < 4; ++blk) trq[blk] = ((16 * role + 4 * (blk ^ q4) + 2 * bb + (p & 1)) << 4) + 8 * (p >> 1);
  f32x16 acc[4];
#pragma unroll
  for (int i = 0; i < 4; ++i) acc[i] = f32x16{};
  const unsigned xr = lds0 + SCR + (4 * qb + (l31 >> 3)) * 1024 + (l31 & 7) * 128;
  const unsigned xsw = (l31 & 7) ^ ((l31 >> 3) & 1) ^ hi;
  bf16x8 xf[2][4];
  static_for<0, 7>([&](auto tc) {
    constexpr int t = decltype(tc)::value;
    top(tc);
    constexpr unsigned sl = SCH.slot[t] * SLOT_BYTES;
    const unsigned rb = lds0 + sl + (8 * hi + q4) * 512;
    if constexpr (t == 0) {
#pragma unroll
      for (int ks = 0; ks < 4; ++ks) xf[0][ks] = lds_b128(xr + (((2 * ks) ^ xsw) << 4));
    }
    pipe<16, 4>(
        [&](auto ic) { constexpr int i = decltype(ic)::value; constexpr int ks = i >> 2, blk = i & 3;
                       return tr_frag(rb + trq[blk] + (16 * ks) * 512, rb + trq[blk] + (16 * ks + 4) * 512); },
        [&](auto ic, bf16x8 a) { constexpr int i = decltype(ic)::value; constexpr int ks = i >> 2, blk = i & 3;
                       acc[blk] = __builtin_amdgcn_mfma_f32_32x32x16_bf16(a, xf[t & 1][ks], acc[blk], 0, 0, 0);
                       if constexpr (t < 6 && i >= 4 && i < 8)
                         xf[(t + 1) & 1][i - 4] = lds_b128(xr + ((t + 1) % NSLOT) * 8192 + (((2 * (i - 4)) ^ xsw) << 4)); });
  });
  bf16x8 qf[16];
#pragma unroll
  for (int blk = 0; blk < 4; ++blk)
#pragma unroll
    for (int sp = 0; sp < 2; ++sp) {
      u32x4 w = {pk2(acc[blk][8 * sp], acc[blk][8 * sp + 1]), pk2(acc[blk][8 * sp + 2], acc[blk][8 * sp + 3]),
                 pk2(acc[blk][8 * sp + 4], acc[blk][8 * sp + 5]), pk2(acc[blk][8 * sp + 6], acc[blk][8 * sp + 7])};
      qf[2 * blk + sp] = __builtin_bit_cast(bf16x8, w);
      *(LAS u32x4*)(lds0 + SCR + wid * 8192 + (2 * blk + sp) * 1024 + lane * 16) = w;
    }

  f32x16 S[8];
  float mx = -3.0e38f;
  static_for<0, 8>([&](auto tc) {
    constexpr int t = decltype(tc)::value;
    top(std::integral_constant<int, 7 + t>{});
    if constexpr (t == 0) {
#pragma unroll
      for (int s = 0; s < 8; ++s) qf[8 + s] = lds_b128(lds0 + SCR + (wid ^ 2) * 8192 + s * 1024 + lane * 16);
    }
    constexpr unsigned sl = SCH.slot[7 + t] * SLOT_BYTES;
    const unsigned kr = lds0 + sl + (32 * role + l31) * 512;
    const unsigned kro = kr + role * 256, krp = kr + (role ^ 1) * 256;
    f32x16 c = f32x16{};
    pipe<16, 4>(
        [&](auto ic) { constexpr int s = decltype(ic)::value; return lds_b128((s >> 3 ? krp : kro) + ((((2 * (s & 7)) + hi) ^ (l31 & 15)) << 4)); },
        [&](auto ic, bf16x8 a) { constexpr int s = decltype(ic)::value;
                       c = __builtin_amdgcn_mfma_f32_32x32x16_bf16(a, qf[s], c, 0, 0, 0); });
    S[t] = c;
#pragma unroll
    for (int r = 0; r < 16; ++r) mx = fmaxf(mx, c[r]);
  });
  mx = swap32_max(mx);
  const unsigned xbuf = lds0 + SCR + 16384;
  *(LAS float*)(xbuf + wid * 256 + lane * 4) = mx;
  lds_sync();
  mx = fmaxf(mx, *(LAS const float*)(xbuf + (wid ^ 2) * 256 + lane * 4));

  constexpr float C = 0.0625f * 1.44269504088896340736f;
  const float mC = -mx * C;
  float lsum = 0.f;
  const unsigned pbuf = lds0 + SCR;
  bf16x8 pown[2];
  auto make_p = [&](const f32x16& s, int par) {
    float pe[16];
#pragma unroll
    for (int r = 0; r < 16; ++r) { pe[r] = __builtin_amdgcn_exp2f(fmaf(s[r], C, mC)); lsum += pe[r]; }
#pragma unroll
    for (int sp = 0; sp < 2; ++sp) {
      u32x4 w = {pk2(pe[8 * sp], pe[8 * sp + 1]), pk2(pe[8 * sp + 2], pe[8 * sp + 3]),
                 pk2(pe[8 * sp + 4], pe[8 * sp + 5]), pk2(pe[8 * sp + 6], pe[8 * sp + 7])};
      pown[sp] = __builtin_bit_cast(bf16x8, w);
      *(LAS u32x4*)(pbuf + par * 8192 + wid * 2048 + sp * 1024 + lane * 16) = w;
    }
  };
  make_p(S[0], 0);

  f32x16 o[4];
#pragma unroll
  for (int i = 0; i < 4; ++i) o[i] = f32x16{};
  static_for<0, 8>([&](auto tc) {
    constexpr int t = decltype(tc)::value;
    top(std::integral_constant<int, 15 + t>{});
    constexpr unsigned sl = SCH.slot[15 + t] * SLOT_BYTES;
    bf16x8 pf[2][2];
    pf[0][0] = pown[0]; pf[0][1] = pown[1];
    pf[1][0] = lds_b128(pbuf + (t & 1) * 8192 + (wid ^ 2) * 2048 + lane * 16);
    pf[1][1] = lds_b128(pbuf + (t & 1) * 8192 + (wid ^ 2) * 2048 + 1024 + lane * 16);
    if constexpr (t == 7) lsum += *(LAS const float*)(xbuf + (wid ^ 2) * 256 + lane * 4);
    const unsigned vr0 = lds0 + sl + (32 * role + 4 * hi + q4) * 512;
    const unsigned vr1 = lds0 + sl + (32 * (role ^ 1) + 4 * hi + q4) * 512;
    float pe[16];
    unsigned pk[8];
    pipe<16, 4>(
        [&](auto ic) { constexpr int i = decltype(ic)::value; constexpr int u = i >> 3, sp = (i >> 2) & 1, blk = i & 3;
                       const unsigned vr = u ? vr1 : vr0;
                       return tr_frag(vr + trc[blk] + (16 * sp) * 512, vr + trc[blk] + (16 * sp + 8) * 512); },
        [&](auto ic, bf16x8 a) { constexpr int i = decltype(ic)::value; constexpr int u = i >> 3, sp = (i >> 2) & 1, blk = i & 3;
                       o[blk] = __builtin_amdgcn_mfma_f32_32x32x16_bf16(a, pf[u][sp], o[blk], 0, 0, 0);
                       if constexpr (t < 7) {
                         pe[i] = __builtin_amdgcn_exp2f(fmaf(S[t + 1][i], C, mC)); lsum += pe[i];
                         if constexpr (i & 1) pk[i >> 1] = pk2(pe[i - 1], pe[i]);
                         if constexpr ((i & 7) == 7) {
                           constexpr int s4 = (i >> 3) * 4;
                           u32x4 w = {pk[s4], pk[s4 + 1], pk[s4 + 2], pk[s4 + 3]};
                           pown[i >> 3] = __builtin_bit_cast(bf16x8, w);
                           *(LAS u32x4*)(pbuf + ((t + 1) & 1) * 8192 + wid * 2048 + (i >> 3) * 1024 + lane * 16) = w;
                         }
                       } });
    if constexpr (t == 6) {
      float l2 = swap32_add(lsum);
      *(LAS float*)(xbuf + wid * 256 + lane * 4) = l2;
      lsum = l2;
    }
  });

  const float inv = 1.0f / lsum;
  lds_sync();
  {
    const unsigned orow = lds0 + SCR + (32 * qb + l31) * 512 + 8 * hi;
#pragma unroll
    for (int blk = 0; blk < 4; ++blk)
#pragma unroll
      for (int rq = 0; rq < 4; ++rq) {
        u32x2 w = {pk2(o[blk][4 * rq] * inv, o[blk][4 * rq + 1] * inv), pk2(o[blk][4 * rq + 2] * inv, o[blk][4 * rq + 3] * inv)};
        *(LAS u32x2*)(orow + (((16 * role + 4 * blk + rq) ^ (l31 & 15)) << 4)) = w;
      }
  }
  int ln = lane;
  asm volatile("" : "+v"(ln));
  const int g4 = ln >> 4, l15 = ln & 15, oq4 = l15 >> 2, op = ln & 3;
  const unsigned tro = (8 * g4 + oq4) * 128 + (((2 * wid + (op >> 1)) ^ ((oq4 >> 1) << 2)) << 4) + 8 * (op & 1);
  char* sp_ = slab + ((size_t)(((h * 8 + b) * 2 + qh) * 4 + wid) * 14) * 1024 + ln * 16;
  bf16x8 of[32];
  f32x4 acc2[2][4];
  const int orot = idx % 7;
  auto store_half = [&](const f32x4* a, int step, int g2) {
    u32x4 w = {pk2(a[2 * g2][0], a[2 * g2][1]), pk2(a[2 * g2][2], a[2 * g2][3]),
               pk2(a[2 * g2 + 1][0], a[2 * g2 + 1][1]), pk2(a[2 * g2 + 1][2], a[2 * g2 + 1][3])};
    const int ot = (step + orot >= 7) ? step + orot - 7 : step + orot;
    __builtin_nontemporal_store(w, (u32x4*)(sp_ + (ot * 2 + g2) * 1024));
  };
  static_for<0, 7>([&](auto tc) {
    constexpr int t = decltype(tc)::value;
    top(std::integral_constant<int, 23 + t>{});
    if constexpr (t == 0) {
#pragma unroll
      for (int s = 0; s < 8; ++s)
#pragma unroll
        for (int nb = 0; nb < 4; ++nb)
          of[nb * 8 + s] = lds_b128(lds0 + SCR + (16 * nb + l15) * 512 + (((4 * s + g4) ^ l15) << 4));
    }
    constexpr unsigned sl = SCH.slot[23 + t] * SLOT_BYTES;
    const unsigned wr = lds0 + sl + tro;
    f32x4 c[4];
#pragma unroll
    for (int nb = 0; nb < 4; ++nb) c[nb] = f32x4{};
    pipe<8, 3>(
        [&](auto ic) { constexpr int i = decltype(ic)::value; return tr_frag(wr + i * 4096, wr + i * 4096 + 512); },
        [&](auto ic, bf16x8 a) { constexpr int i = decltype(ic)::value;
#pragma unroll
                       for (int nb = 0; nb < 4; ++nb) c[nb] = __builtin_amdgcn_mfma_f32_16x16x32_bf16(a, of[nb * 8 + i], c[nb], 0, 0, 0);
                       if constexpr (t > 0 && i == 1) store_half(acc2[(t - 1) & 1], t - 1, 0);
                       if constexpr (t > 0 && i == 3) store_half(acc2[(t - 1) & 1], t - 1, 1); });
#pragma unroll
    for (int nb = 0; nb < 4; ++nb) acc2[t & 1][nb] = c[nb];
  });
  store_half(acc2[0], 6, 0);
  store_half(acc2[0], 6, 1);
}

__global__ __launch_bounds__(256) void reduce_kernel(const u32x4* __restrict__ slab, char* __restrict__ out) {
  const int i = blockIdx.x * 256 + threadIdx.x;
  float s[8];
#pragma unroll
  for (int k = 0; k < 8; ++k) s[k] = 0.f;
  u32x4 v[16];
#pragma unroll
  for (int h = 0; h < 16; ++h) v[h] = __builtin_nontemporal_load(&slab[(size_t)h * 57344 + i]);
#pragma unroll
  for (int h = 0; h < 16; ++h)
#pragma unroll
    for (int k = 0; k < 4; ++k) {
      s[2 * k] += __uint_as_float(v[h][k] << 16);
      s[2 * k + 1] += __uint_as_float(v[h][k] & 0xffff0000u);
    }
  const int lane = i & 63, g2 = (i >> 6) & 1;
  int rest = i >> 7;
  const int nb = rest % 7; rest /= 7;
  const int wid = rest & 3, qh = (rest >> 2) & 1, b = rest >> 3;
  const int row = b * 128 + qh * 64 + 32 * g2 + (lane & 15);
  const int n0 = 64 * nb + 16 * wid + 4 * (lane >> 4);
  char* o = out + ((size_t)row * 448 + n0) * 2;
  u32x2 w0 = {pk2(s[0], s[1]), pk2(s[2], s[3])};
  u32x2 w1 = {pk2(s[4], s[5]), pk2(s[6], s[7])};
  *(u32x2*)o = w0;
  *(u32x2*)(o + 16 * 896) = w1;
}

extern "C" void kernel_launch(void* const* d_in, const int* in_sizes, int n_in, void* d_out, int out_size,
                              void* d_ws, size_t ws_size, hipStream_t stream) {
  const char* x = (const char*)d_in[0];
  const char* k = (const char*)d_in[1];
  const char* v = (const char*)d_in[2];
  const char* wq = (const char*)d_in[3];
  const char* wo = (const char*)d_in[4];
  char* slab = (char*)d_ws;
  attn_kernel<<<256, 512, 0, stream>>>(x, k, v, wq, wo, slab);
  reduce_kernel<<<224, 256, 0, stream>>>((const u32x4*)slab, (char*)d_out);
}
```

```cpp
#include <hip/hip_runtime.h>
#include <hip/hip_bf16.h>
#include <stdint.h>
#include <type_traits>

typedef __bf16 bf16x8 __attribute__((ext_vector_type(8)));
typedef __bf16 bf16x2 __attribute__((ext_vector_type(2)));
typedef short s16x4 __attribute__((ext_vector_type(4)));
typedef short s16x8 __attribute__((ext_vector_type(8)));
typedef float f32x2 __attribute__((ext_vector_type(2)));
typedef float f32x16 __attribute__((ext_vector_type(16)));
typedef unsigned u32x2 __attribute__((ext_vector_type(2)));
typedef unsigned u32x4 __attribute__((ext_vector_type(4)));
#define LAS __attribute__((address_space(3)))
#define DI __device__ __forceinline__

constexpr int NSTEP = 30;
constexpr int NSLOT = 4;
constexpr unsigned SLOT_BYTES = 32768;
constexpr unsigned SCR = NSLOT * SLOT_BYTES;

struct Sched { int slot[NSTEP]; int wait[NSTEP]; };
constexpr Sched make_sched() {
  Sched s{};
  int cum = 0;
  int endT[NSTEP] = {}; int endX[8] = {};
  auto X = [&](int j) { cum += 2; endX[j] = cum; };
  auto T = [&](int t) { cum += 8; endT[t] = cum; };
  X(0); T(0); X(1); T(1);
  for (int t = 0; t < NSTEP; ++t) {
    s.slot[t] = t % NSLOT;
    int need = endT[t];
    if (t + 1 <= 6 && endX[t + 1] > need) need = endX[t + 1];
    s.wait[t] = cum - need;
    if (t == 0) { X(2); T(2); X(3); T(3); }
    else if (t + 3 < NSTEP) { if (t + 3 <= 6) X(t + 3); T(t + 3); }
  }
  return s;
}
constexpr Sched SCH = make_sched();

template <int B, int E, class F> DI void static_for(F&& f) {
  if constexpr (B < E) { f(std::integral_constant<int, B>{}); static_for<B + 1, E>(f); }
}

template <int N, int D, class L, class M> DI void pipe(L&& load, M&& mma) {
  bf16x8 fr[N];
  static_for<0, D>([&](auto ic) { fr[decltype(ic)::value] = load(ic); });
  __builtin_amdgcn_sched_barrier(0);
  static_for<0, N>([&](auto ic) {
    constexpr int i = decltype(ic)::value;
    if constexpr (i + D < N) fr[i + D] = load(std::integral_constant<int, i + D>{});
    mma(ic, fr[i]);
    __builtin_amdgcn_sched_barrier(0);
  });
}

DI void dma16(unsigned lds_addr, const char* sbase, unsigned voff) {
  asm volatile("s_mov_b32 m0, %0\n\ts_nop 0\n\tglobal_load_lds_dwordx4 %1, %2"
               :: "s"(lds_addr), "v"(voff), "s"(sbase) : "memory");
}
DI void dma16nt(unsigned lds_addr, const char* sbase, unsigned voff) {
  asm volatile("s_mov_b32 m0, %0\n\ts_nop 0\n\tglobal_load_lds_dwordx4 %1, %2 nt"
               :: "s"(lds_addr), "v"(voff), "s"(sbase) : "memory");
}
template <int N> DI void top_sync() {
  asm volatile("s_waitcnt vmcnt(%0) lgkmcnt(0)\n\ts_barrier" :: "n"(N) : "memory");
}
DI void lds_sync() { asm volatile("s_waitcnt lgkmcnt(0)\n\ts_barrier" ::: "memory"); }

DI unsigned pk2(float a, float b) {
  f32x2 v = {a, b};
  bf16x2 r = __builtin_convertvector(v, bf16x2);
  return __builtin_bit_cast(unsigned, r);
}
DI bf16x8 tr_frag(unsigned a0, unsigned a1) {
  s16x4 lo = __builtin_amdgcn_ds_read_tr16_b64_v4i16((LAS s16x4*)a0);
  s16x4 hi = __builtin_amdgcn_ds_read_tr16_b64_v4i16((LAS s16x4*)a1);
  s16x8 r = __builtin_shufflevector(lo, hi, 0, 1, 2, 3, 4, 5, 6, 7);
  return __builtin_bit_cast(bf16x8, r);
}
DI bf16x8 lds_b128(unsigned a) { return *(LAS const bf16x8*)a; }
DI float swap32_max(float v) {
  auto rr = __builtin_amdgcn_permlane32_swap(__float_as_uint(v), __float_as_uint(v), false, false);
  return fmaxf(__uint_as_float(rr[0]), __uint_as_float(rr[1]));
}
DI float swap32_add(float v) {
  auto rr = __builtin_amdgcn_permlane32_swap(__float_as_uint(v), __float_as_uint(v), false, false);
  return __uint_as_float(rr[0]) + __uint_as_float(rr[1]);
}

__global__ __launch_bounds__(512, 2) void attn_kernel(const char* __restrict__ x, const char* __restrict__ Kc,
                                                      const char* __restrict__ Vc, const char* __restrict__ wq,
                                                      const char* __restrict__ wo, char* __restrict__ slab) {
  __shared__ __attribute__((aligned(1024))) char smem[163840];
  const unsigned lds0 = (unsigned)(size_t)smem;
  const int tid = threadIdx.x, lane = tid & 63;
  const int wid8 = __builtin_amdgcn_readfirstlane(tid >> 6);
  const int wid = wid8 & 3;
  const int qb = wid & 1, role = wid >> 1;
  const int hi = lane >> 5, l31 = lane & 31;
  const int bid = blockIdx.x;
  const int xcd = bid & 7, idx = bid >> 3;
  const int qh = idx & 1, hl = (idx >> 1) & 1, b = idx >> 2, h = xcd * 2 + hl;

  if (wid8 >= 4) {
    const size_t kvo = (size_t)(b * 16 + h) * (512 * 512) + (size_t)((wid >> 1) * 32 + (wid & 1) * 16) * 512;
    const char* Kb = Kc + kvo;
    const char* Vb = Vc + kvo;
    const int rot = b;
    const char* Wqb = wq + (size_t)(wid * 16) * 8192 + h * 512;
    const char* Wob = wo + (size_t)(h * 256 + wid * 64) * 896;
    const char* Xb = x + (size_t)(b * 128 + qh * 64 + wid * 16) * 896;
    const unsigned ldsw = lds0 + wid * 8192;
    const int krot = idx % 7;
    auto issue_x = [&](auto sc) {
      constexpr int S = decltype(sc)::value;
      const int kt = (S + krot >= 7) ? S + krot - 7 : S + krot;
      static_for<0, 2>([&](auto jc) {
        constexpr int j = decltype(jc)::value;
        unsigned voff = (lane >> 3) * 896 + (((lane & 7) ^ (lane >> 3) ^ j) << 4);
        dma16(lds0 + SCR + (S % NSLOT) * 8192 + (2 * wid + j) * 1024, Xb + (size_t)(8 * j) * 896 + 128 * kt, voff);
      });
    };
    auto issue = [&](auto sc) {
      constexpr int S = decltype(sc)::value;
      constexpr unsigned sl = SCH.slot[S] * SLOT_BYTES;
      const int kt = (S + krot >= 7) ? S + krot - 7 : S + krot;
      static_for<0, 8>([&](auto jc) {
        constexpr int j = decltype(jc)::value;
        if constexpr (S < 7) {
          unsigned voff = hi * 8192 + ((l31 ^ ((2 * (j & 1) + hi) << 2)) << 4);
          dma16(ldsw + sl + j * 1024, Wqb + (size_t)(64 * kt + 2 * j) * 8192, voff);
        } else if constexpr (S < 15) {
          unsigned voff = hi * 512 + ((l31 ^ hi ^ (2 * j)) << 4);
          dma16nt(ldsw + sl + j * 1024, Kb + (size_t)(64 * ((S - 7 + rot) & 7) + 2 * j) * 512, voff);
        } else if constexpr (S < 23) {
          unsigned voff = hi * 512 + ((l31 ^ ((2 * (j & 1) + hi) << 2)) << 4);
          dma16nt(ldsw + sl + j * 1024, Vb + (size_t)(64 * ((S - 15 + rot) & 7) + 2 * j) * 512, voff);
        } else {
          unsigned voff = (lane >> 3) * 896 + (((lane & 7) ^ (((lane >> 4) & 1) << 2)) << 4);
          const int ot = (S - 23 + krot >= 7) ? S - 23 + krot - 7 : S - 23 + krot;
          dma16(ldsw + sl + j * 1024, Wob + (size_t)(8 * j) * 896 + 128 * ot, voff);
        }
      });
    };
#define IC(v) std::integral_constant<int, (v)>{}
    issue_x(IC(0)); issue(IC(0)); issue_x(IC(1)); issue(IC(1));
    static_for<0, NSTEP>([&](auto sc) {
      constexpr int S = decltype(sc)::value;
      top_sync<SCH.wait[S]>();
      if constexpr (S == 0) { issue_x(IC(2)); issue(IC(2)); issue_x(IC(3)); issue(IC(3)); }
      else if constexpr (S + 3 < NSTEP) {
        if constexpr (S + 3 <= 6) issue_x(IC(S + 3));
        issue(IC(S + 3));
      }
      if constexpr (S == 14 || S == 22) { asm volatile("s_barrier" ::: "memory"); }
    });
    return;
  }

  auto top = [&](auto sc) { lds_sync(); };

  const int q4 = (lane & 15) >> 2, p = lane & 3, bb = (lane >> 4) & 1;
  unsigned trc[4];
#pragma unroll
  for (int blk = 0; blk < 4; ++blk) trc[blk] = ((16 * role + 4 * (blk ^ q4) + 2 * bb + (p >> 1)) << 4) + 8 * (p & 1);

  unsigned trq[4];
#pragma unroll
  for (int blk = 0; blk < 4; ++blk) trq[blk] = ((16 * role + 4 * (blk ^ q4) + 2 * bb + (p & 1)) << 4) + 8 * (p >> 1);
  f32x16 acc[4];
#pragma unroll
  for (int i = 0; i < 4; ++i) acc[i] = f32x16{};
  const unsigned xr = lds0 + SCR + (4 * qb + (l31 >> 3)) * 1024 + (l31 & 7) * 128;
  const unsigned xsw = (l31 & 7) ^ ((l31 >> 3) & 1) ^ hi;
  bf16x8 xf[2][4];
  static_for<0, 7>([&](auto tc) {
    constexpr int t = decltype(tc)::value;
    top(tc);
    constexpr unsigned sl = SCH.slot[t] * SLOT_BYTES;
    const unsigned rb = lds0 + sl + (8 * hi + q4) * 512;
    if constexpr (t == 0) {
#pragma unroll
      for (int ks = 0; ks < 4; ++ks) xf[0][ks] = lds_b128(xr + (((2 * ks) ^ xsw) << 4));
    }
    pipe<16, 4>(
        [&](auto ic) { constexpr int i = decltype(ic)::value; constexpr int ks = i >> 2, blk = i & 3;
                       return tr_frag(rb + trq[blk] + (16 * ks) * 512, rb + trq[blk] + (16 * ks + 4) * 512); },
        [&](auto ic, bf16x8 a) { constexpr int i = decltype(ic)::value; constexpr int ks = i >> 2, blk = i & 3;
                       acc[blk] = __builtin_amdgcn_mfma_f32_32x32x16_bf16(a, xf[t & 1][ks], acc[blk], 0, 0, 0);
                       if constexpr (t < 6 && i >= 4 && i < 8)
                         xf[(t + 1) & 1][i - 4] = lds_b128(xr + ((t + 1) % NSLOT) * 8192 + (((2 * (i - 4)) ^ xsw) << 4)); });
  });
  bf16x8 qf[16];
#pragma unroll
  for (int blk = 0; blk < 4; ++blk)
#pragma unroll
    for (int sp = 0; sp < 2; ++sp) {
      u32x4 w = {pk2(acc[blk][8 * sp], acc[blk][8 * sp + 1]), pk2(acc[blk][8 * sp + 2], acc[blk][8 * sp + 3]),
                 pk2(acc[blk][8 * sp + 4], acc[blk][8 * sp + 5]), pk2(acc[blk][8 * sp + 6], acc[blk][8 * sp + 7])};
      qf[2 * blk + sp] = __builtin_bit_cast(bf16x8, w);
      *(LAS u32x4*)(lds0 + SCR + wid * 8192 + (2 * blk + sp) * 1024 + lane * 16) = w;
    }

  f32x16 S[8];
  float mx = -3.0e38f;
  static_for<0, 8>([&](auto tc) {
    constexpr int t = decltype(tc)::value;
    top(std::integral_constant<int, 7 + t>{});
    if constexpr (t == 0) {
#pragma unroll
      for (int s = 0; s < 8; ++s) qf[8 + s] = lds_b128(lds0 + SCR + (wid ^ 2) * 8192 + s * 1024 + lane * 16);
    }
    constexpr unsigned sl = SCH.slot[7 + t] * SLOT_BYTES;
    const unsigned kr = lds0 + sl + (32 * role + l31) * 512;
    const unsigned kro = kr + role * 256, krp = kr + (role ^ 1) * 256;
    f32x16 c = f32x16{};
    pipe<16, 4>(
        [&](auto ic) { constexpr int s = decltype(ic)::value; return lds_b128((s >> 3 ? krp : kro) + ((((2 * (s & 7)) + hi) ^ (l31 & 15)) << 4)); },
        [&](auto ic, bf16x8 a) { constexpr int s = decltype(ic)::value;
                       c = __builtin_amdgcn_mfma_f32_32x32x16_bf16(a, qf[s], c, 0, 0, 0); });
    S[t] = c;
#pragma unroll
    for (int r = 0; r < 16; ++r) mx = fmaxf(mx, c[r]);
  });
  mx = swap32_max(mx);
  const unsigned xbuf = lds0 + SCR + 16384;
  *(LAS float*)(xbuf + wid * 256 + lane * 4) = mx;
  lds_sync();
  mx = fmaxf(mx, *(LAS const float*)(xbuf + (wid ^ 2) * 256 + lane * 4));

  constexpr float C = 0.0625f * 1.44269504088896340736f;
  const float mC = -mx * C;
  float lsum = 0.f;
  const unsigned pbuf = lds0 + SCR;
  bf16x8 pown[2];
  auto make_p = [&](const f32x16& s, int par) {
    float pe[16];
#pragma unroll
    for (int r = 0; r < 16; ++r) { pe[r] = __builtin_amdgcn_exp2f(fmaf(s[r], C, mC)); lsum += pe[r]; }
#pragma unroll
    for (int sp = 0; sp < 2; ++sp) {
      u32x4 w = {pk2(pe[8 * sp], pe[8 * sp + 1]), pk2(pe[8 * sp + 2], pe[8 * sp + 3]),
                 pk2(pe[8 * sp + 4], pe[8 * sp + 5]), pk2(pe[8 * sp + 6], pe[8 * sp + 7])};
      pown[sp] = __builtin_bit_cast(bf16x8, w);
      *(LAS u32x4*)(pbuf + par * 8192 + wid * 2048 + sp * 1024 + lane * 16) = w;
    }
  };
  make_p(S[0], 0);

  f32x16 o[4];
#pragma unroll
  for (int i = 0; i < 4; ++i) o[i] = f32x16{};
  static_for<0, 8>([&](auto tc) {
    constexpr int t = decltype(tc)::value;
    top(std::integral_constant<int, 15 + t>{});
    constexpr unsigned sl = SCH.slot[15 + t] * SLOT_BYTES;
    bf16x8 pf[2][2];
    pf[0][0] = pown[0]; pf[0][1] = pown[1];
    pf[1][0] = lds_b128(pbuf + (t & 1) * 8192 + (wid ^ 2) * 2048 + lane * 16);
    pf[1][1] = lds_b128(pbuf + (t & 1) * 8192 + (wid ^ 2) * 2048 + 1024 + lane * 16);
    if constexpr (t == 7) lsum += *(LAS const float*)(xbuf + (wid ^ 2) * 256 + lane * 4);
    const unsigned vr0 = lds0 + sl + (32 * role + 4 * hi + q4) * 512;
    const unsigned vr1 = lds0 + sl + (32 * (role ^ 1) + 4 * hi + q4) * 512;
    float pe[16];
    unsigned pk[8];
    pipe<16, 4>(
        [&](auto ic) { constexpr int i = decltype(ic)::value; constexpr int u = i >> 3, sp = (i >> 2) & 1, blk = i & 3;
                       const unsigned vr = u ? vr1 : vr0;
                       return tr_frag(vr + trc[blk] + (16 * sp) * 512, vr + trc[blk] + (16 * sp + 8) * 512); },
        [&](auto ic, bf16x8 a) { constexpr int i = decltype(ic)::value; constexpr int u = i >> 3, sp = (i >> 2) & 1, blk = i & 3;
                       o[blk] = __builtin_amdgcn_mfma_f32_32x32x16_bf16(a, pf[u][sp], o[blk], 0, 0, 0);
                       if constexpr (t < 7) {
                         pe[i] = __builtin_amdgcn_exp2f(fmaf(S[t + 1][i], C, mC)); lsum += pe[i];
                         if constexpr (i & 1) pk[i >> 1] = pk2(pe[i - 1], pe[i]);
                         if constexpr ((i & 7) == 7) {
                           constexpr int s4 = (i >> 3) * 4;
                           u32x4 w = {pk[s4], pk[s4 + 1], pk[s4 + 2], pk[s4 + 3]};
                           pown[i >> 3] = __builtin_bit_cast(bf16x8, w);
                           *(LAS u32x4*)(pbuf + ((t + 1) & 1) * 8192 + wid * 2048 + (i >> 3) * 1024 + lane * 16) = w;
                         }
                       } });
    if constexpr (t == 6) {
      float l2 = swap32_add(lsum);
      *(LAS float*)(xbuf + wid * 256 + lane * 4) = l2;
      lsum = l2;
    }
  });

  bf16x8 of[16];
#pragma unroll
  for (int blk = 0; blk < 4; ++blk)
#pragma unroll
    for (int sp = 0; sp < 2; ++sp) {
      u32x4 w = {pk2(o[blk][8 * sp], o[blk][8 * sp + 1]), pk2(o[blk][8 * sp + 2], o[blk][8 * sp + 3]),
                 pk2(o[blk][8 * sp + 4], o[blk][8 * sp + 5]), pk2(o[blk][8 * sp + 6], o[blk][8 * sp + 7])};
      of[blk * 2 + sp] = __builtin_bit_cast(bf16x8, w);
    }
  const float inv = 1.0f / lsum;
  lds_sync();
#pragma unroll
  for (int s = 0; s < 8; ++s) *(LAS u32x4*)(lds0 + SCR + wid * 8192 + s * 1024 + lane * 16) = __builtin_bit_cast(u32x4, of[s]);

  int ln = lane;
  asm volatile("" : "+v"(ln));
  const unsigned tro = (((4 * role + 2 * ((ln >> 4) & 1) + ((ln & 3) >> 1)) ^ ((((ln & 15) >> 2) >> 1) << 2)) << 4) + 8 * (ln & 1) + (4 * (ln >> 5) + ((ln & 15) >> 2)) * 128;
  char* sp_ = slab + ((size_t)(((h * 8 + b) * 2 + qh) * 4 + wid) * 14) * 1024 + ln * 16;
  f32x16 acc2[2];
  const int orot = idx % 7;
  auto store_half = [&](const f32x16& a, int step, int g2) {
    u32x4 w = {pk2(a[8 * g2] * inv, a[8 * g2 + 1] * inv), pk2(a[8 * g2 + 2] * inv, a[8 * g2 + 3] * inv),
               pk2(a[8 * g2 + 4] * inv, a[8 * g2 + 5] * inv), pk2(a[8 * g2 + 6] * inv, a[8 * g2 + 7] * inv)};
    const int ot = (step + orot >= 7) ? step + orot - 7 : step + orot;
    __builtin_nontemporal_store(w, (u32x4*)(sp_ + (ot * 2 + g2) * 1024));
  };
  static_for<0, 7>([&](auto tc) {
    constexpr int t = decltype(tc)::value;
    top(std::integral_constant<int, 23 + t>{});
    if constexpr (t == 0) {
#pragma unroll
      for (int s = 0; s < 8; ++s) of[8 + s] = lds_b128(lds0 + SCR + (wid ^ 2) * 8192 + s * 1024 + lane * 16);
    }
    constexpr unsigned sl = SCH.slot[23 + t] * SLOT_BYTES;
    const unsigned wro = lds0 + sl + tro + role * 16384, wrp = lds0 + sl + tro + (role ^ 1) * 16384;
    f32x16 c = f32x16{};
    pipe<16, 4>(
        [&](auto ic) { constexpr int i = decltype(ic)::value; const unsigned wr = (i >> 3) ? wrp : wro;
                       return tr_frag(wr + (i & 7) * 2048, wr + (i & 7) * 2048 + 1024); },
        [&](auto ic, bf16x8 a) { constexpr int i = decltype(ic)::value;
                       c = __builtin_amdgcn_mfma_f32_32x32x16_bf16(a, of[i], c, 0, 0, 0);
                       if constexpr (t > 0 && i == 2) store_half(acc2[(t - 1) & 1], t - 1, 0);
                       if constexpr (t > 0 && i == 6) store_half(acc2[(t - 1) & 1], t - 1, 1); });
    acc2[t & 1] = c;
  });
  store_half(acc2[0], 6, 0);
  store_half(acc2[0], 6, 1);
}

__global__ __launch_bounds__(256) void reduce_kernel(const u32x4* __restrict__ slab, char* __restrict__ out) {
  const int i = blockIdx.x * 256 + threadIdx.x;
  float s[8];
#pragma unroll
  for (int k = 0; k < 8; ++k) s[k] = 0.f;
  u32x4 v[16];
#pragma unroll
  for (int h = 0; h < 16; ++h) v[h] = __builtin_nontemporal_load(&slab[(size_t)h * 57344 + i]);
#pragma unroll
  for (int h = 0; h < 16; ++h)
#pragma unroll
    for (int k = 0; k < 4; ++k) {
      s[2 * k] += __uint_as_float(v[h][k] << 16);
      s[2 * k + 1] += __uint_as_float(v[h][k] & 0xffff0000u);
    }
  const int lane = i & 63, g2 = (i >> 6) & 1;
  int rest = i >> 7;
  const int nb = rest % 7; rest /= 7;
  const int wid = rest & 3, qh = (rest >> 2) & 1, b = rest >> 3;
  const int row = b * 128 + qh * 64 + (wid & 1) * 32 + (lane & 31);
  const int n0 = 32 * (2 * nb + (wid >> 1)) + 16 * g2 + 4 * (lane >> 5);
  char* o = out + ((size_t)row * 448 + n0) * 2;
  u32x2 w0 = {pk2(s[0], s[1]), pk2(s[2], s[3])};
  u32x2 w1 = {pk2(s[4], s[5]), pk2(s[6], s[7])};
  *(u32x2*)o = w0;
  *(u32x2*)(o + 16) = w1;
}

extern "C" void kernel_launch(void* const* d_in, const int* in_sizes, int n_in, void* d_out, int out_size,
                              void* d_ws, size_t ws_size, hipStream_t stream) {
  const char* x = (const char*)d_in[0];
  const char* k = (const char*)d_in[1];
  const char* v = (const char*)d_in[2];
  const char* wq = (const char*)d_in[3];
  const char* wo = (const char*)d_in[4];
  char* slab = (char*)d_ws;
  attn_kernel<<<256, 512, 0, stream>>>(x, k, v, wq, wo, slab);
  reduce_kernel<<<224, 256, 0, stream>>>((const u32x4*)slab, (char*)d_out);
}
```
